```python
import torch

def _softmax(x, axis=-1):
    return torch.softmax(x, dim=axis)

def _gelu(x):
    return torch.nn.functional.gelu(x, approximate='tanh')

def _silu(x):
    return torch.nn.functional.silu(x)

def _sigmoid(x):
    return torch.sigmoid(x)

def _layernorm(x, axis=-1, eps=1e-05):
    m = x.mean(dim=axis, keepdim=True)
    v = x.var(dim=axis, keepdim=True, unbiased=False)
    return (x - m) / torch.sqrt(v + eps)

def _scatter_add(tgt, idx, src, axis=0):
    return tgt.clone().index_add_(axis, idx, src)

def _segment_sum(src, seg, n):
    out = torch.zeros((n,) + src.shape[1:], dtype=src.dtype)
    return out.index_add_(0, seg, src)

def _pad(x, pw):
    flat = tuple((v for pair in reversed(pw) for v in pair))
    return torch.nn.functional.pad(x, flat)
kernel_name = 'standalone__conformer_conv_module__speech'

def setup_inputs():

    _w = lambda a: (lambda t: t.to(torch.bfloat16) if t.is_floating_point() else t)(torch.as_tensor(a))
    B, T, D, K = (2, 4000, 512, 31)
    return {'x': _w((lambda *s: torch.randn(*s, dtype=torch.bfloat16) * 0.2)(B, T, D)), 'w_pw1': _w((lambda *s: torch.randn(*s, dtype=torch.bfloat16) * 0.2)(D, 2 * D)), 'dw': _w((lambda *s: torch.randn(*s, dtype=torch.bfloat16) * 0.2)(K, D)), 'w_pw2': _w((lambda *s: torch.randn(*s, dtype=torch.bfloat16) * 0.05)(D, D))}

def reference(x, w_pw1, dw, w_pw2):
    B, T, D = x.shape
    K = dw.shape[0]
    h = x @ w_pw1
    a, b = (h[..., :D].clone(), h[..., D:].clone())
    h = a * _sigmoid(b)
    pad = K // 2
    hp = _pad(h, ((0, 0), (pad, pad), (0, 0)))
    out = torch.zeros_like(h)
    for i in range(K):
        out = out + hp[:, i:i + T, :] * dw[i][None, None, :]
    return _silu(out) @ w_pw2
```

```cpp
#include <hip/hip_runtime.h>
#include <hip/hip_bf16.h>
typedef __hip_bfloat16 bf16;
constexpr int B_=2,T_=4000,D_=512,K_=31;
__device__ __forceinline__ float b2f(bf16 v){return __bfloat162float(v);}
__global__ void k1(const bf16* x,const bf16* w1,bf16* h){
  int c=blockIdx.x*64+threadIdx.x; int r=blockIdx.y*4+threadIdx.y;
  float a=0,g=0;
  for(int k=0;k<D_;k++){float xv=b2f(x[r*D_+k]);a+=xv*b2f(w1[k*2*D_+c]);g+=xv*b2f(w1[k*2*D_+D_+c]);}
  h[r*D_+c]=__float2bfloat16(a/(1.f+__expf(-g)));
}
__global__ void k2(const bf16* h,const bf16* dw,bf16* s){
  int c=blockIdx.x*64+threadIdx.x; int r=blockIdx.y*4+threadIdx.y; int b=r/T_,t=r%T_;
  float a=0;
  for(int i=0;i<K_;i++){int tt=t+i-15; if(tt>=0&&tt<T_) a+=b2f(h[(b*T_+tt)*D_+c])*b2f(dw[i*D_+c]);}
  s[r*D_+c]=__float2bfloat16(a/(1.f+__expf(-a)));
}
__global__ void k3(const bf16* s,const bf16* w2,bf16* o){
  int c=blockIdx.x*64+threadIdx.x; int r=blockIdx.y*4+threadIdx.y;
  float a=0;
  for(int k=0;k<D_;k++){a+=b2f(s[r*D_+k])*b2f(w2[k*D_+c]);}
  o[r*D_+c]=__float2bfloat16(a);
}
extern "C" void kernel_launch(void* const* d_in, const int* in_sizes, int n_in,
                              void* d_out, int out_size, void* d_ws, size_t ws_size,
                              hipStream_t stream) {
  const bf16* x=(const bf16*)d_in[0]; const bf16* w1=(const bf16*)d_in[1]; const bf16* dw=(const bf16*)d_in[2]; const bf16* w2=(const bf16*)d_in[3];
  bf16* h=(bf16*)d_ws; bf16* s=h+B_*T_*D_;
  dim3 g(D_/64,B_*T_/4), b(64,4);
  k1<<<g,b,0,stream>>>(x,w1,h);
  k2<<<g,b,0,stream>>>(h,dw,s);
  k3<<<g,b,0,stream>>>(s,w2,(bf16*)d_out);
}
```

```cpp
#include <hip/hip_runtime.h>
#include <hip/hip_bf16.h>
typedef __hip_bfloat16 bf16;
constexpr int B_=2,T_=4000,D_=512,K_=31;
#define LAS __attribute__((address_space(3)))
__device__ __forceinline__ unsigned long long rt(){return __builtin_amdgcn_s_memrealtime();}
template<int N> __device__ __forceinline__ void waitvm(){ asm volatile("s_waitcnt vmcnt(%0)"::"n"(N):"memory"); }
#define ROT 0
__global__ __launch_bounds__(512) void probe(const char* x,const char* w1){
  extern __shared__ __attribute__((aligned(16))) char smem[];
  constexpr int INFLU=4;
  unsigned long long t0=rt();
  int bid=blockIdx.x; int g,j,gi;
  {int xcd=bid&7,i=bid>>3; gi=i>>3; g=xcd*4+gi; j=i&7;}
  int wave=__builtin_amdgcn_readfirstlane(threadIdx.x>>6), lane=threadIdx.x&63;
  int b=g>>4, tile=g&15; int row0=tile*250-15;
  auto rx=__builtin_amdgcn_make_buffer_rsrc((void*)(x+(size_t)b*4096000),0,4096000,0x00020000);
  auto rw=__builtin_amdgcn_make_buffer_rsrc((void*)w1,0,1<<20,0x00020000);
  int vx=lane*16 + (row0+wave*4)*1024;
  int vw=(wave*16+(lane>>4))*2048+((lane&15)>>3)*1024+j*128+(lane&7)*16;
  constexpr int SEQ[13]={16,17,18,19,0,1,2,3,4,5,6,7,8};
  #pragma unroll
  for(int u=0;u<13;u++){
    int e=SEQ[u];
    LAS char* dst=(LAS char*)smem+(u%INFLU)*32768+wave*4096;
    if(e<16){ int blk=ROT?(e+j):e; if(blk>=9) blk-=9;
      #pragma unroll
      for(int i=0;i<4;i++) __builtin_amdgcn_raw_ptr_buffer_load_lds(rx,dst+i*1024,16,vx+blk*32768+i*1024,0,0,0);
    } else { int q=ROT?((e-16+gi)&3):(e-16);
      #pragma unroll
      for(int i=0;i<4;i++) __builtin_amdgcn_raw_ptr_buffer_load_lds(rw,dst+i*1024,16,vw,q*128*2048+i*4*2048,0,0);
    }
    if(u>=INFLU-1){ waitvm<(INFLU-1)*4>(); }
  }
  waitvm<0>();
  __syncthreads();
  unsigned long long t1=rt();
  if(bid==100){ unsigned long long until=t1+(t1-t0)*100; while(rt()<until) __builtin_amdgcn_s_sleep(10); }
}
__device__ __forceinline__ float b2f(bf16 v){return __bfloat162float(v);}
__global__ void k1(const bf16* x,const bf16* w1,bf16* h){
  int c=blockIdx.x*64+threadIdx.x; int r=blockIdx.y*4+threadIdx.y;
  float a=0,g=0;
  for(int k=0;k<D_;k++){float xv=b2f(x[r*D_+k]);a+=xv*b2f(w1[k*2*D_+c]);g+=xv*b2f(w1[k*2*D_+D_+c]);}
  h[r*D_+c]=__float2bfloat16(a/(1.f+__expf(-g)));
}
__global__ void k2(const bf16* h,const bf16* dw,bf16* s){
  int c=blockIdx.x*64+threadIdx.x; int r=blockIdx.y*4+threadIdx.y; int b=r/T_,t=r%T_;
  float a=0;
  for(int i=0;i<K_;i++){int tt=t+i-15; if(tt>=0&&tt<T_) a+=b2f(h[(b*T_+tt)*D_+c])*b2f(dw[i*D_+c]);}
  s[r*D_+c]=__float2bfloat16(a/(1.f+__expf(-a)));
}
__global__ void k3(const bf16* s,const bf16* w2,bf16* o){
  int c=blockIdx.x*64+threadIdx.x; int r=blockIdx.y*4+threadIdx.y;
  float a=0;
  for(int k=0;k<D_;k++){a+=b2f(s[r*D_+k])*b2f(w2[k*D_+c]);}
  o[r*D_+c]=__float2bfloat16(a);
}
extern "C" void kernel_launch(void* const* d_in, const int* in_sizes, int n_in,
                              void* d_out, int out_size, void* d_ws, size_t ws_size,
                              hipStream_t stream) {
  const bf16* x=(const bf16*)d_in[0]; const bf16* w1=(const bf16*)d_in[1]; const bf16* dw=(const bf16*)d_in[2]; const bf16* w2=(const bf16*)d_in[3];
  bf16* h=(bf16*)d_ws; bf16* s=h+B_*T_*D_;
  dim3 g(D_/64,B_*T_/4), b(64,4);
  hipFuncSetAttribute((const void*)probe,hipFuncAttributeMaxDynamicSharedMemorySize,160*1024);
  probe<<<256,512,128*1024,stream>>>((const char*)x,(const char*)w1);
  k1<<<g,b,0,stream>>>(x,w1,h);
  k2<<<g,b,0,stream>>>(h,dw,s);
  k3<<<g,b,0,stream>>>(s,w2,(bf16*)d_out);
}
```

```cpp
#include <hip/hip_runtime.h>
#include <hip/hip_bf16.h>

typedef short s16x4 __attribute__((ext_vector_type(4)));
typedef __bf16 bf16x8 __attribute__((ext_vector_type(8)));
typedef __bf16 bf16x2 __attribute__((ext_vector_type(2)));
typedef float f32x4 __attribute__((ext_vector_type(4)));
typedef float f32x2 __attribute__((ext_vector_type(2)));
typedef unsigned u32x4 __attribute__((ext_vector_type(4)));
typedef unsigned u32x2 __attribute__((ext_vector_type(2)));
typedef unsigned long long u64;
#define LAS __attribute__((address_space(3)))
#define GAS __attribute__((address_space(1)))
#define DEVI __device__ __forceinline__
#define AI __attribute__((always_inline))
#define RLX_AGENT __ATOMIC_RELAXED, __HIP_MEMORY_SCOPE_AGENT

constexpr int T_ = 4000, TILE = 250;
constexpr int SLOT = 16896;
constexpr int NS = 7;
constexpr int HT_OFF = NS * SLOT;
constexpr int HS = 528;
constexpr int HREG = 16 * HS;
constexpr int TAB_OFF = HT_OFF + 64 * HS;
constexpr int TS = 176;
constexpr int MISC_OFF = TAB_OFF + 64 * TS;
constexpr int LDS_BYTES = 160 * 1024;
static_assert(MISC_OFF + 512 <= LDS_BYTES && 64 * HS >= 2 * SLOT);
constexpr size_t WS_FLAG = 8u << 20;
constexpr size_t WS_EPOCH = WS_FLAG + 32 * 4 * 8 * 128;
constexpr size_t WS_STAMP = WS_EPOCH + 32 * 128;
constexpr unsigned CODE_LINES = 208;
constexpr int PF = 3;

#define KROT gi
#define WA warmx(0, 2, 0, 4);
#define WB warmx(0, 2, 4, 4); warmx(64, 2, 0, 8);
#define WBN 2
#define WC warmx(128, 2, 0, 8);
#define WCN 1
#define WD warmx(192, 3, 0, 8);
#define WDN 1
#define WE warmv();
#define WEN 1
#define PSL 2
#define FST 1
#define CST
template <int N> DEVI void waitvm() { asm volatile("s_waitcnt vmcnt(%0)" ::"n"(N) : "memory"); }
template <int N> DEVI void waitlgkm() { asm volatile("s_waitcnt lgkmcnt(%0)" ::"n"(N) : "memory"); }
DEVI void bar() { asm volatile("" ::: "memory"); __builtin_amdgcn_s_barrier(); asm volatile("" ::: "memory"); }
DEVI void sfence() { __builtin_amdgcn_sched_barrier(0); }
DEVI unsigned pk2(float lo, float hi) { f32x2 p = {lo, hi}; return __builtin_bit_cast(unsigned, __builtin_convertvector(p, bf16x2)); }
DEVI bf16x8 ldsA(LAS char* p) { return *(LAS bf16x8*)p; }
DEVI s16x4 ld64(LAS char* p) { return *(LAS s16x4*)p; }
DEVI s16x4 ldtr(LAS char* p) { return __builtin_amdgcn_ds_read_tr16_b64_v4i16((LAS s16x4*)p); }
DEVI bf16x8 cat8(s16x4 a, s16x4 b) { return __builtin_bit_cast(bf16x8, __builtin_shufflevector(a, b, 0, 1, 2, 3, 4, 5, 6, 7)); }
DEVI void mf(f32x4& c, const bf16x8& a, const bf16x8& b) { asm volatile("v_mfma_f32_16x16x32_bf16 %0, %1, %2, %0" : "+v"(c) : "v"(a), "v"(b)); }
DEVI void mf0(f32x4& c, const bf16x8& a, const bf16x8& b) { asm volatile("v_mfma_f32_16x16x32_bf16 %0, %1, %2, 0" : "=&v"(c) : "v"(a), "v"(b)); }
DEVI float vnl2e(float a) { float r; asm volatile("v_mul_f32 %0, 0xbfb8aa3b, %1" : "=v"(r) : "v"(a)); return r; }
DEVI float vadd1(float a) { float r; asm volatile("v_add_f32 %0, 1.0, %1" : "=v"(r) : "v"(a)); return r; }
DEVI float vmul(float a, float b) { float r; asm volatile("v_mul_f32 %0, %1, %2" : "=v"(r) : "v"(a), "v"(b)); return r; }
DEVI float vexp2(float a) { float r; asm volatile("v_exp_f32 %0, %1" : "=v"(r) : "v"(a)); return r; }
DEVI float vrcp(float a) { float r; asm volatile("v_rcp_f32 %0, %1" : "=v"(r) : "v"(a)); return r; }
DEVI unsigned vpk(float a, float b) { unsigned r; asm volatile("v_cvt_pk_bf16_f32 %0, %1, %2" : "=v"(r) : "v"(a), "v"(b)); return r; }
DEVI LAS char* opaque(LAS char* p) { asm volatile("" : "+v"(p)); return p; }
DEVI void lds_inc(unsigned addr) { asm volatile("ds_add_u32 %0, %1" ::"v"(addr), "v"(1u) : "memory"); }
DEVI unsigned lds_inc_rtn(unsigned addr) { unsigned r; asm volatile("ds_add_rtn_u32 %0, %1, %2\n\ts_waitcnt lgkmcnt(0)" : "=&v"(r) : "v"(addr), "v"(1u) : "memory"); return r; }
DEVI unsigned umin_(unsigned a, unsigned b) { return a < b ? a : b; }
DEVI unsigned umax_(unsigned a, unsigned b) { return a > b ? a : b; }
template <int N> DEVI void nops() { asm volatile("s_nop %0" ::"n"(N)); }

template <int V> struct IC { static constexpr int value = V; };
template <int B, int E, class F> DEVI void sfor(F&& f) {
  if constexpr (B < E) { f(IC<B>{}); sfor<B + 1, E>(f); }
}
#define CI(name, arg) constexpr int name = decltype(arg)::value
template <int NB> struct BaseN {
  LAS char* p[NB];
  DEVI BaseN(LAS char* b) {
    p[0] = b;
#pragma unroll
    for (int i = 1; i < NB; i++) p[i] = opaque(b + i * 61440);
  }
  template <int OFF> DEVI LAS char* at() const {
    constexpr int k = OFF / 61440 < NB ? OFF / 61440 : NB - 1;
    static_assert(OFF - k * 61440 < 65536);
    return p[k] + (OFF - k * 61440);
  }
};
typedef BaseN<2> Base2;
typedef BaseN<3> Base3;

#define ST(i) do {} while (0)
#define STA do {} while (0)
#define STDUMP() do {} while (0)

constexpr int wunit(int m) { return m < 4 ? m : m + 2; }
constexpr int slot_of(int n) {
  return n < 7 ? n * SLOT : n < 9 ? HT_OFF + (n - 7) * SLOT : n < 31 ? ((n - 2) % NS) * SLOT : n < 33 ? slot_of(n - 5) : slot_of(25);
}
constexpr int sbuf(int P, int h) { return slot_of(P == 1 ? 29 + h : P == 2 ? 26 + h : h ? 24 : 28); }
constexpr int sfrag(int P, int hf, int ks) { return sbuf(P, ks >> 3) + (hf * 8 + (ks & 7)) * 1024; }
constexpr int PFS_OFF = MISC_OFF + 256;

__global__ __launch_bounds__(512, 2) void conv_module_kernel(const char* __restrict__ x, const char* __restrict__ w1,
                                                             const char* __restrict__ dw, const char* __restrict__ w2,
                                                             char* __restrict__ out, char* __restrict__ ws) {
  extern __shared__ __attribute__((aligned(16))) char smem_[];
  LAS char* smem = (LAS char*)smem_;
  u64 pc0;
  asm volatile("s_getpc_b64 %0" : "=s"(pc0));
  const int bid = blockIdx.x;
  const int gi = bid >> 6, j = (bid >> 3) & 7, g = (bid & 7) * 4 + gi;
  const int wave = __builtin_amdgcn_readfirstlane(threadIdx.x >> 6);
  const int lane = threadIdx.x & 63;
  const int batch = g >> 4, t0 = (g & 15) * TILE;
  const int cg = j & 3, rh = j >> 2;
  const int q = lane >> 4, l15 = lane & 15;
  const unsigned misc = (unsigned)(size_t)(smem + MISC_OFF);
  GAS u64* epoch = (GAS u64*)(ws + WS_EPOCH + g * 128);
  ST(0);
  const auto rs = __builtin_amdgcn_make_buffer_rsrc((void*)(ws + (size_t)g * 262144), 0, 262144, 0x00020000);
  GAS u64* flags = (GAS u64*)(ws + WS_FLAG + (size_t)g * 4096);
  const int svo = ((l15 >> 2) * 16 + 2 * j + ((wave >> 1) & 1)) * 1024 + ((2 * (wave & 1) + (q >> 1)) * 16 + 2 * (l15 & 3) + (q & 1)) * 16;

  const int w3 = wave & 3;
  const auto rw2 = __builtin_amdgcn_make_buffer_rsrc((void*)w2, 0, 512 * 1024, 0x00020000);
  const int vv = (4 * w3 + q) * 1024 + cg * 256 + (((l15 >> 2) ^ q) * 64) + (lane & 3) * 16;
  auto issueV = [&](int n, int i) {
    __builtin_amdgcn_raw_ptr_buffer_load_lds(rw2, smem + slot_of(n) + w3 * 1024 + i * 4096, 16, vv, ((n - 26) * 64 + 16 * i) * 1024, 0, 0);
  };
  u32x4 sf[2][8];
  auto sload1 = [&](auto PP, auto I) AI {
    CI(P, PP); CI(i, I);
    sf[P & 1][i] = __builtin_amdgcn_raw_buffer_load_b128(rs, lane * 16 + w3 * 1024, ((4 * P + 2 * rh + ((i & 3) >> 1)) * 16 + 8 * (i >> 2) + 4 * (i & 1)) * 1024, 16);
  };
  auto sload = [&](auto PP) AI { sfor<0, 8>([&](auto I) AI { sload1(PP, I); }); };
  auto flagld = [&](int P) { return __hip_atomic_load(flags + P * 32 + (lane & 7) * FST, RLX_AGENT); };
  auto flagck = [&](int P, u64 v, u64 tag) AI {
    if (!__all(v == tag)) {
      unsigned spins = 0;
#pragma unroll 1
      do { __builtin_amdgcn_s_sleep(PSL); v = flagld(P); } while (!__all(v == tag) && ++spins < (1u << 20));
    }
    asm volatile("" ::: "memory");
  };
  auto signal = [&](int w) {
    waitlgkm<0>();
    if (lane == 0) lds_inc(misc + w * 4);
  };
  auto await = [&](int w, unsigned target = 4) {
    volatile LAS unsigned* words = (volatile LAS unsigned*)(smem + MISC_OFF);
#pragma unroll 1
    for (unsigned spins = 0; spins < (1u << 24); spins++) {
      if ((unsigned)__builtin_amdgcn_readfirstlane(words[w]) == target) break;
      __builtin_amdgcn_s_sleep(1);
    }
    asm volatile("" ::: "memory");
  };

  if (wave < 4) {
    const int ng = wave;
    __builtin_amdgcn_s_setprio(2);
    if (ng == 1) {
      const auto rc = __builtin_amdgcn_make_buffer_rsrc((void*)pc0, 0, CODE_LINES * 128, 0x00020000);
      __builtin_amdgcn_raw_ptr_buffer_load_lds(rc, smem + PFS_OFF, 4, ((bid >> 3) * 8 + (lane & 7)) * 128, 0, 0, 0);
    }
    const auto rout = __builtin_amdgcn_make_buffer_rsrc((void*)(out + (size_t)(batch * T_ + t0) * 1024), 0, TILE * 1024, 0x00020000);
    const u64 tag = __hip_atomic_load(epoch, RLX_AGENT) + 0x9E3779B97F4A7C15ull;
    {
      const auto rdw = __builtin_amdgcn_make_buffer_rsrc((void*)dw, 0, 31 * 1024, 0x00020000);
      unsigned tap[11];
      const int vo = (10 * ng - 4) * 1024 + (64 * j + lane) * 2;
#pragma unroll
      for (int m = 0; m < 11; m++) {
        int o = vo + m * 1024;
        asm volatile("" : "+v"(o));
        tap[m] = __builtin_amdgcn_raw_buffer_load_b16(rdw, o, 0, 0);
      }
      LAS unsigned* tb = (LAS unsigned*)(smem + TAB_OFF + lane * TS);
#pragma unroll
      for (int m = 0; m < 10; m++) tb[5 * ng + (m & 1) * 20 + (m >> 1)] = tap[m] | (tap[m + 1] << 16);
      tb[40 + ng] = 0u;
      if (threadIdx.x < 16) *(LAS unsigned*)(smem + MISC_OFF + threadIdx.x * 4) = 0u;
      if (threadIdx.x == 0) *(LAS u64*)(smem + MISC_OFF + 64) = tag;
    }
    const Base2 abase(smem + (l15 >> 1) * 1024 + (l15 >> 2) * 64 + (l15 & 1) * 512 + ((q ^ (l15 & 3)) * 16));
    constexpr int RH = 8 * 1024 + 4 * 64;
    const int q4 = l15 >> 2, p4 = lane & 3;
    const Base3 wa(smem + (8 * q + q4) * 256 + ((ng ^ q4 ^ ((q & 1) << 2)) * 32) + p4 * 8);
    const Base3 wg(smem + (8 * q + q4) * 256 + (((4 + ng) ^ q4 ^ ((q & 1) << 2)) * 32) + p4 * 8);
    LAS char* hbase = opaque(smem + HT_OFF + (ng * 16 + l15) * HS + q * 16);
    LAS char* hwrap = opaque(hbase - (q > 0 ? 256 : 0));
    bf16x8 Ba[16], Bg[16];
    bf16x8 A[PF][2];
    f32x4 ca[3][2], cgt[3][2];
    float e[8];
    u32x4 hp;

    auto loadW = [&](auto R) {
      CI(r, R);
      constexpr int ks = r >> 1, off = slot_of(wunit(ks >> 1)) + (ks & 1) * 8192;
      if constexpr (r & 1) Bg[ks] = cat8(ldtr(wg.at<off>()), ldtr(wg.at<off + 1024>()));
      else Ba[ks] = cat8(ldtr(wa.at<off>()), ldtr(wa.at<off + 1024>()));
    };
    auto glu = [&](auto U, auto PP, auto BP) {
      CI(u0, U); CI(pp, PP); CI(bp, BP);
      constexpr int u = u0 == 0 ? 0 : u0 == 1 ? -1 : u0 - 1;
      if constexpr (u >= 0 && u < 4) { e[2 * u] = vnl2e(cgt[pp][u >> 1][(2 * u) & 3]); e[2 * u + 1] = vnl2e(cgt[pp][u >> 1][(2 * u + 1) & 3]); }
      if constexpr (u >= 4 && u < 12) e[u - 4] = vexp2(e[u - 4]);
      if constexpr (u >= 12 && u < 16) { e[2 * u - 24] = vadd1(e[2 * u - 24]); e[2 * u - 23] = vadd1(e[2 * u - 23]); }
      if constexpr (u >= 16 && u < 24) e[u - 16] = vrcp(e[u - 16]);
      if constexpr (u >= 24 && u < 28) {
        constexpr int i = 2 * u - 48;
        e[i] = vmul(e[i], ca[pp][i >> 2][i & 3]); e[i + 1] = vmul(e[i + 1], ca[pp][i >> 2][(i & 3) + 1]);
      }
      if constexpr (u == 28 || u == 29) { constexpr int i = 2 * u - 56; hp[i] = vpk(e[2 * i], e[2 * i + 1]); hp[i + 1] = vpk(e[2 * i + 2], e[2 * i + 3]); }
      constexpr int t = (bp * 32) & 127;
      if constexpr (u == 30) *(LAS u32x4*)(hbase + t * 2) = hp;
      if constexpr (u == 32) {
        if constexpr (t == 64) {
          *(LAS u32x2*)(hwrap + 256 + 248) = (u32x2){hp[0], hp[1]};
          *(LAS u32x2*)(hbase + 256) = (u32x2){hp[2], hp[3]};
        } else {
          *(LAS u32x2*)(hbase + 256 + ((t + 60) & 127) * 2) = (u32x2){hp[0], hp[1]};
          *(LAS u32x2*)(hbase + 256 + ((t + 60) & 127) * 2 + 8) = (u32x2){hp[2], hp[3]};
        }
      }
    };
    auto step = [&](auto I, auto PP, auto KS, auto FIRST, auto NOFF, auto&& f0, auto&& f1) {
      CI(i, I); CI(pp, PP); CI(ks, KS); CI(first, FIRST); CI(noff, NOFF);
      if constexpr (first) mf0(ca[pp][0], A[i][0], Ba[ks]); else mf(ca[pp][0], A[i][0], Ba[ks]);
      sfence(); f0(); sfence();
      if constexpr (first) mf0(cgt[pp][0], A[i][0], Bg[ks]); else mf(cgt[pp][0], A[i][0], Bg[ks]);
      sfence();
      if constexpr (noff >= 0) A[i][0] = ldsA(abase.at<noff>());
      sfence();
      if constexpr (first) mf0(cgt[pp][1], A[i][1], Bg[ks]); else mf(cgt[pp][1], A[i][1], Bg[ks]);
      sfence(); f1(); sfence();
      if constexpr (first) mf0(ca[pp][1], A[i][1], Ba[ks]); else mf(ca[pp][1], A[i][1], Ba[ks]);
      sfence();
      if constexpr (noff >= 0) A[i][1] = ldsA(abase.at<noff + RH>());
      sfence();
    };
    auto nop = [] {};
    auto primeA = [&](auto N) {
      CI(n, N);
      sfor<0, PF>([&](auto I) { CI(i, I); A[i][0] = ldsA(abase.at<slot_of(n) + i * 64>()); A[i][1] = ldsA(abase.at<slot_of(n) + i * 64 + RH>()); });
    };

    waitlgkm<0>();
    bar();
    bar();
    ST(1);
    sfor<0, 4>(loadW);
    primeA(IC<4>{});
    sfor<0, 16>([&](auto S) {
      CI(s, S);
      constexpr int n = s + PF;
      step(IC<s % PF>{}, IC<(s >> 3)>{}, IC<(s & 7)>{}, IC<(s & 7) == 0>{}, IC<(n < 16 ? slot_of(4 + (n >> 3)) + (n & 7) * 64 : -1)>{},
           [&] { if constexpr (s < 6) loadW(IC<2 * s + 4>{}); },
           [&] { if constexpr (s < 6) loadW(IC<2 * s + 5>{}); });
    });
    ST(2);
    bar();
    bar();
    ST(3);
    sfor<16, 20>(loadW);
    primeA(IC<10>{});
    sfor<0, 16>([&](auto S) {
      CI(s, S);
      constexpr int n = s + PF;
      if constexpr (s == 8 - PF || s == 16 - PF) bar();
      step(IC<s % PF>{}, IC<(s >> 3)>{}, IC<8 + (s & 7)>{}, IC<0>{}, IC<slot_of(10 + (n >> 3)) + (n & 7) * 64>{},
           [&] { if constexpr (s < 6) loadW(IC<2 * s + 20>{});
                 if constexpr (s == 8) nops<7>();
                 if constexpr (s >= 8) { glu(IC<4 * (s - 8)>{}, IC<0>{}, IC<0>{}); glu(IC<4 * (s - 8) + 1>{}, IC<0>{}, IC<0>{}); } },
           [&] { if constexpr (s < 6) loadW(IC<2 * s + 21>{});
                 if constexpr (s >= 8) { glu(IC<4 * (s - 8) + 2>{}, IC<0>{}, IC<0>{}); glu(IC<4 * (s - 8) + 3>{}, IC<0>{}, IC<0>{}); } });
    });
    ST(5);
    sfor<2, 7>([&](auto BB) {
      CI(b, BB);
      sfor<0, 16>([&](auto S) {
        CI(s, S);
        constexpr int n = s + PF, gs = 16 * (b - 1) + s;
        if constexpr (s == 3) { waitlgkm<(b & 3) == 0 ? 4 : 4>(); bar(); }
        CST
        auto fill = [&](auto G) {
          CI(gp, G);
          if constexpr (b > 2 || gp > 0) glu(IC<gp + 31>{}, IC<(b & 1)>{}, IC<b - 2>{});
          glu(IC<gp - 1>{}, IC<(b & 1) ^ 1>{}, IC<b - 1>{});
        };
        step(IC<gs % PF>{}, IC<(b & 1)>{}, IC<s>{}, IC<s == 0>{}, IC<slot_of(8 + 2 * b + (n >> 3)) + (n & 7) * 64>{},
             [&] { fill(IC<2 * s>{}); }, [&] { fill(IC<2 * s + 1>{}); });
      });
    });
    const Base2 vb(smem + (8 * q + q4) * 256 + ((ng ^ q4) * 64) + p4 * 16);
    auto loadV = [&](auto R) {
      CI(r, R);
      constexpr int ks = r >> 1, off = slot_of(26 + (ks >> 1)) + (ks & 1) * 8192 + (r & 1) * 8;
      if constexpr (r & 1) Bg[ks] = cat8(ldtr(vb.at<off>()), ldtr(vb.at<off + 1024>()));
      else Ba[ks] = cat8(ldtr(vb.at<off>()), ldtr(vb.at<off + 1024>()));
    };
    sfor<0, 32>([&](auto S) {
      CI(s, S);
      constexpr int n = s + PF, gs = 96 + s, set = 1 + ((s >> 3) & 1), ks = (s >> 4) * 8 + (s & 7);
      if constexpr (s == 3 || s == 19) { waitlgkm<4>(); bar(); }
      if constexpr (s == 27) { waitlgkm<5>(); if (lane == 0) lds_inc(misc + 10 * 4); }
      auto fill = [&](auto G) {
        CI(gp, G);
        glu(IC<gp + 31>{}, IC<1>{}, IC<5>{});
        glu(IC<gp - 1>{}, IC<0>{}, IC<6>{});
        if constexpr (gp >= 38 && gp < 50) loadV(IC<gp - 38>{});
        if constexpr (gp >= 50) glu(IC<gp - 50>{}, IC<1>{}, IC<7>{});
      };
      step(IC<gs % PF>{}, IC<set>{}, IC<ks>{}, IC<ks == 0>{}, IC<(n < 32 ? slot_of(22 + (n >> 3)) + (n & 7) * 64 : -1)>{},
           [&] { fill(IC<2 * s>{}); }, [&] { fill(IC<2 * s + 1>{}); });
    });
    ST(6);
    u64 fv = flagld(0);
    sfor<14, 32>([&](auto U) { glu(U, IC<1>{}, IC<7>{}); });
    volatile LAS unsigned* words = (volatile LAS unsigned*)(smem + MISC_OFF);
    unsigned fl = words[8];
    sfor<0, 31>([&](auto U) { CI(u, U); glu(IC<u + 32>{}, IC<1>{}, IC<7>{}); glu(U, IC<2>{}, IC<8>{}); });
    if (__builtin_amdgcn_readfirstlane(fl) != 4) await(8);
    asm volatile("" ::: "memory");
    sfor<31, 34>([&](auto U) { glu(U, IC<2>{}, IC<8>{}); });
    waitlgkm<0>();
    bar();
    ST(7);
    const Base2 sbase(smem + lane * 16);
    const int tt = 4 * ((l15 >> 1) & 3) + 2 * (l15 >> 3) + (l15 & 1);
    const int ovo = (rh * 32 + tt) * 1024 + cg * 256 + ng * 64 + q * 16;
    unsigned op[4];
    auto ostep = [&](auto U, auto PP, auto PC) {
      CI(u, U); CI(pp, PP); CI(pc, PC);
      constexpr int hf = u / 3, s = u % 3;
      if constexpr (u < 6) {
        if constexpr (s == 0) { op[0] = vpk(ca[pp][hf][0], ca[pp][hf][1]); op[1] = vpk(ca[pp][hf][2], ca[pp][hf][3]); }
        if constexpr (s == 1) { op[2] = vpk(cgt[pp][hf][0], cgt[pp][hf][1]); op[3] = vpk(cgt[pp][hf][2], cgt[pp][hf][3]); }
        if constexpr (s == 2) __builtin_amdgcn_raw_buffer_store_b128((u32x4){op[0], op[1], op[2], op[3]}, rout, ovo + (pc * 64 + hf * 16) * 1024, 0, 0);
      }
    };
    __builtin_amdgcn_s_setprio(0);
    const Base2 swb(smem + ng * 1024 + lane * 16);
    auto swrite1 = [&](auto PP, auto I) { CI(P, PP); CI(i, I); *(LAS u32x4*)(swb.at<sbuf(P, i >> 2) + (i & 3) * 4096>()) = sf[P & 1][i]; };
    flagck(0, fv, tag);
    sload(IC<0>{});
    fv = flagld(1);
    sfor<12, 20>(loadV);
    sfence();
    sfor<0, 4>([&](auto I) { CI(i, I); issueV(33, i); });
    flagck(1, fv, tag);
    ST(14);
    sfor<0, 8>([&](auto I) { swrite1(IC<0>{}, I); sload1(IC<1>{}, I); sfence(); });
    waitvm<8>();
    signal(4);
    await(4, 8);
    ST(8);
    sfor<0, PF>([&](auto I) { CI(i, I); A[i][0] = ldsA(sbase.at<sfrag(0, 0, i)>()); A[i][1] = ldsA(sbase.at<sfrag(0, 1, i)>()); });
    sfor<0, 64>([&](auto S) {
      CI(gs, S);
      constexpr int P = gs >> 4, s = gs & 15, i = gs % PF, pp = P & 1, n = gs + PF;
      constexpr int idx = (n == 16 || n == 32 || n == 48) ? (n >> 4) : -1;
      constexpr int n2 = n + 1, idx2 = (n2 == 16 || n2 == 32 || n2 == 48) ? (n2 >> 4) : -1;
      if constexpr (idx >= 0) {
        if (__builtin_amdgcn_readfirstlane(fl) != 4) await(4 + idx);
        asm volatile("" ::: "memory");
        if constexpr (idx > 0) ST(8 + idx);
      }
      if constexpr (s == 0) mf0(ca[pp][0], Ba[s], A[i][0]); else mf(ca[pp][0], Ba[s], A[i][0]);
      sfence();
      if constexpr (P > 0 && s >= 2) ostep(IC<s - 2>{}, IC<pp ^ 1>{}, IC<P - 1>{});
      if constexpr (gs < 6) loadV(IC<20 + 2 * gs>{});
      if constexpr (gs >= 6 && gs < 10) swrite1(IC<1>{}, IC<2 * gs - 12>{});
      if constexpr (idx2 >= 0) fl = words[4 + idx2];
      sfence();
      if constexpr (s == 0) mf0(cgt[pp][0], Bg[s], A[i][0]); else mf(cgt[pp][0], Bg[s], A[i][0]);
      sfence();
      if constexpr (n < 64) A[i][0] = ldsA(sbase.at<sfrag((n & 63) >> 4, 0, n & 15)>());
      sfence();
      if constexpr (s == 0) mf0(cgt[pp][1], Bg[s], A[i][1]); else mf(cgt[pp][1], Bg[s], A[i][1]);
      sfence();
      if constexpr (gs < 6) loadV(IC<21 + 2 * gs>{});
      if constexpr (gs >= 6 && gs < 10) swrite1(IC<1>{}, IC<2 * gs - 11>{});
      if constexpr (gs == 11) { waitlgkm<4>(); if (lane == 0) lds_inc(misc + 5 * 4); }
      if constexpr (gs == 17) { if (lane == 0) lds_inc(misc + 9 * 4); }
      sfence();
      if constexpr (s == 0) mf0(ca[pp][1], Ba[s], A[i][1]); else mf(ca[pp][1], Ba[s], A[i][1]);
      sfence();
      if constexpr (n < 64) A[i][1] = ldsA(sbase.at<sfrag((n & 63) >> 4, 1, n & 15)>());
      sfence();
    });
    ST(12);
    nops<7>();
    sfor<0, 6>([&](auto U) { ostep(U, IC<1>{}, IC<3>{}); nops<1>(); });
    ST(13);
    STDUMP();
  } else {
    const int hw = wave - 4;
    const auto rx = __builtin_amdgcn_make_buffer_rsrc((void*)(x + (size_t)batch * (T_ * 1024)), 0, T_ * 1024, 0x00020000);
    const auto rw1 = __builtin_amdgcn_make_buffer_rsrc((void*)w1, 0, 1024 * 1024, 0x00020000);
    const int lc = (l15 >> 1) ^ q ^ ((hw >> 1) << 2);
    const int vw = (4 * hw + q) * 2048 + (lc >> 2) * 1024 + j * 128 + (lc & 3) * 32 + (lane & 1) * 16;
    const int vx = (t0 - 15 + 8 * (hw >> 1) + 2 * (hw & 1) + (lane >> 5)) * 1024 + (((((lane & 31) ^ (2 * (hw & 1) + (lane >> 5))) * 16) + KROT * 128) & 511);
    const int vx1 = vx + 32768;
    auto issue1 = [&](int n, int i) {
      if (n < 4 || (n >= 6 && n < 10)) {
        const int m = n < 4 ? n : n - 2;
        __builtin_amdgcn_raw_ptr_buffer_load_lds(rw1, smem + slot_of(n) + hw * 1024 + i * 4096, 16, vw, (((m & 4) + ((m + KROT) & 3)) * 64 + 16 * i) * 2048, 0, 0);
      } else if (n < 26) {
        const int b = n == 4 || n == 10 ? 0 : n == 5 || n == 11 ? 1 : n < 22 ? (n - 8) >> 1 : 7 + (n & 1);
        const int half = n < 6 ? 0 : n < 12 ? 1 : n < 22 ? (n & 1) : (n - 22) >> 1;
        LAS char* dst = smem + slot_of(n) + hw * 1024 + (hw >> 1) * 64 + i * (4096 + 128);
        if (b == 0) __builtin_amdgcn_raw_ptr_buffer_load_lds(rx, dst, 16, vx + half * 512 + (16 * (i & 1) + 4 * (i >> 1)) * 1024, 0, 0, 0);
        else __builtin_amdgcn_raw_ptr_buffer_load_lds(rx, dst, 16, vx1, (b - 1) * 32768 + half * 512 + (16 * (i & 1) + 4 * (i >> 1)) * 1024, 0, 0);
      } else {
        issueV(n, i);
      }
    };
    auto issue = [&](int n) {
#pragma unroll
      for (int i = 0; i < 4; i++) issue1(n, i);
    };
    auto warmx = [&](int r0, int rpw, int l0, int nl) {
      const int r = lane / nl;
      const int o = r < rpw ? (t0 - 15 + r0 + (4 * j + hw) * rpw + r) * 1024 + (l0 + lane % nl) * 128 : -1;
      __builtin_amdgcn_raw_ptr_buffer_load_lds(rx, smem + PFS_OFF, 4, o, 0, 0, 0);
    };
    auto warmv = [&] {
      __builtin_amdgcn_raw_ptr_buffer_load_lds(rw2, smem + PFS_OFF, 4, lane < 32 ? (((bid >> 3) * 4 + hw) * 32 + lane) * 128 : -1, 0, 0, 0);
    };
    STA;
    issue(0);
    WA
    issue(1); issue(2); issue(3); issue(4); issue(5); issue(6);
    WB
    bar();
    const u64 tag = *(LAS u64*)(smem + MISC_OFF + 64);
    bf16x8 T1[16], T2[4];
    {
      const int e4 = l15 >> 2, b3 = lane & 3;
      LAS char* tb = smem + TAB_OFF + 16 * hw * TS;
      const int i1 = (b3 & 1) * 20 + ((8 * q - b3 + 4) >> 1), i2 = (b3 & 1) * 20 + ((36 - b3) >> 1);
      sfor<0, 4>([&](auto EE) {
        CI(ee, EE);
        LAS unsigned* p = (LAS unsigned*)opaque(tb + 4 * ee * TS + (e4 == ee ? i1 : 40) * 4);
        sfor<0, 4>([&](auto QD) {
          CI(qd, QD);
          const u32x4 v = {p[qd * 44], p[qd * 44 + 1], p[qd * 44 + 2], p[qd * 44 + 3]};
          sfence();
          T1[4 * ee + qd] = __builtin_bit_cast(bf16x8, v);
        });
      });
      LAS unsigned* p = (LAS unsigned*)opaque(tb + 4 * q * TS + (e4 == q ? i2 : 40) * 4);
      sfor<0, 4>([&](auto QD) { CI(qd, QD); const u32x4 v = {0u, 0u, p[qd * 44], p[qd * 44 + 1]}; sfence(); T2[qd] = __builtin_bit_cast(bf16x8, v); });
    }
    LAS char* c1[2]; LAS char* c2[2];
    sfor<0, 2>([&](auto PP) {
      CI(pp, PP);
      const int sh = (l15 & 1) * 60 + 64 * pp + 4 * l15;
      c1[pp] = opaque(smem + HT_OFF + hw * HREG + (l15 & 1) * 256 + ((sh + 8 * q) & 127) * 2);
      const int op = (l15 & 1) ^ 1;
      c2[pp] = opaque(smem + HT_OFF + hw * HREG + 4 * q * HS + op * 256 + ((op * 60 + 64 * pp + 4 * l15 + 28) & 127) * 2);
    });
    waitvm<4 + WBN>();
    STA;
    bar();
    STA;
    issue(7); issue(8);
    STA;
    bar();
    STA;
    issue(9); issue(10); issue(11); issue(12);
    waitvm<8>();
    STA;
    bar();
    STA;
    issue(13);
    waitvm<8>();
    STA;
    bar();
    STA;
    issue(14);
    WC
    waitvm<8 + WCN>();
    STA;
    bar();
    STA;
    issue(15); issue(16);
    waitvm<8 + WCN>();
    STA;
    bar();
    STA;
    issue(17); issue(18);
    WD
    waitvm<8 + WDN>();
    STA;
    bar();
    STA;
    issue(19); issue(20);
    WE
    f32x4 d[4];
    unsigned sp[4][2];
    bf16x8 W[20];
    float sv[8], stt[8];
    auto convrd1 = [&](auto PP, auto K) {
      CI(P, PP); CI(k, K);
      constexpr int hh = k / 10, ee = (k % 10) >> 1, qq = k & 1, qd = 2 * hh + qq;
      W[10 * hh + 5 * qq + ee] = ldsA(ee < 4 ? c1[P & 1] + (4 * ee + qd) * HS : c2[P & 1] + qd * HS);
      sfence();
    };
    auto lo = [] { sfence(); __builtin_amdgcn_s_setprio(0); sfence(); };
    auto hi = [] { sfence(); __builtin_amdgcn_s_setprio(3); sfence(); };
    auto convmm = [&](auto HH, auto&& f) {
      CI(hh, HH);
      hi();
      sfor<0, 10>([&](auto K) {
        CI(k, K);
        constexpr int ee = k >> 1, qq = k & 1, qd = 2 * hh + qq;
        const bf16x8& t = ee < 4 ? T1[(4 * ee + qd) & 15] : T2[qd];
        d[qd] = __builtin_amdgcn_mfma_f32_16x16x32_bf16(t, W[10 * hh + 5 * qq + ee], ee == 0 ? (f32x4){0.f, 0.f, 0.f, 0.f} : d[qd], 0, 0, 0);
        sfence(); f(K); sfence();
      });
      sfence();
    };
    auto none = [](auto) {};
    auto shadowEA = [&](auto K) { CI(k, K); if constexpr (k < 8) stt[k] = vexp2(stt[k]); else { stt[2 * k - 16] = vadd1(stt[2 * k - 16]); stt[2 * k - 15] = vadd1(stt[2 * k - 15]); } };
    auto phM = [&](auto HH) { CI(hh, HH); sfor<0, 8>([&](auto I) { CI(i, I); sv[i] = d[2 * hh + (i & 1)][i >> 1]; stt[i] = sv[i] * -1.4426950408889634f; sfence(); }); };
    auto head = [&](auto PP, auto&& g) AI {
      sfor<0, 10>([&](auto K) { convrd1(PP, K); g(K); sfence(); });
      convmm(IC<0>{}, [&](auto K) { CI(k, K); convrd1(PP, IC<10 + k>{}); });
      phM(IC<0>{}); convmm(IC<1>{}, shadowEA); lo();
    };
    auto phE = [&](auto&& g) { sfor<0, 8>([&](auto I) { CI(i, I); stt[i] = vexp2(stt[i]); g(I); sfence(); }); };
    auto phA = [&](auto B0) { CI(b0, B0); sfor<b0, 8>([&](auto I) { CI(i, I); stt[i] = vadd1(stt[i]); }); sfence(); };
    auto phR = [&](auto&& g) { sfor<0, 8>([&](auto I) { CI(i, I); stt[i] = vrcp(stt[i]); g(I); sfence(); }); };
    auto phF = [&](auto HH) {
      CI(hh, HH);
      sfor<0, 8>([&](auto I) { CI(i, I); stt[i] = vmul(stt[i], sv[i]); });
      sfor<0, 4>([&](auto R) { CI(r, R); sp[r][hh] = pk2(stt[2 * r], stt[2 * r + 1]); });
      sfence();
    };
    auto dma = [&](int n) { return [&issue1, n](auto I) { CI(i, I); if constexpr (i & 1) issue1(n, i >> 1); }; };
    auto sstore = [&](auto PP) {
      CI(P, PP);
      sfor<0, 2>([&](auto I) {
        CI(i, I);
        auto r0 = __builtin_amdgcn_permlane16_swap(sp[2 * i][0], sp[2 * i + 1][0], false, false);
        auto r1 = __builtin_amdgcn_permlane16_swap(sp[2 * i][1], sp[2 * i + 1][1], false, false);
        __builtin_amdgcn_raw_buffer_store_b128((u32x4){r0[0], r1[0], r0[1], r1[1]}, rs, svo + P * 65536 + i * 128, 0, 16);
      });
    };
    const Base2 swb(smem + hw * 1024 + lane * 16);
    auto swrite1 = [&](auto PP, auto I) { CI(P, PP); CI(i, I); *(LAS u32x4*)(swb.at<sbuf(P, i >> 2) + (i & 3) * 4096>()) = sf[P & 1][i]; };
    auto dma8 = [&](int n) { return [&issue1, n](auto I) { CI(i, I); issue1(n + (i >> 2), i & 3); }; };
    auto dma3 = [&](int n) { return [&issue1, n](auto K) { CI(k, K); if constexpr (k == 1 || k == 4 || k == 7) issue1(n, 1 + k / 3); }; };
    auto flagst = [&](int P) { if (threadIdx.x == 256) __hip_atomic_store(flags + P * 32 + j * FST, tag, RLX_AGENT); };
    auto publish = [&](int P) {
      if (lane == 0) {
        if (lds_inc_rtn(misc + P * 4) == 3) __hip_atomic_store(flags + P * 32 + j * FST, tag, RLX_AGENT);
      }
    };
    auto dmah = [&](int n) { return [&issue1, n](auto K) { CI(k, K); if constexpr (k < 8) issue1(n + (k >> 2), k & 3); }; };
    waitvm<8 + WDN + WEN>();
    STA;
    bar();
    STA;
    head(IC<0>{}, dmah(21));
    phA(IC<4>{}); phR(none); phF(IC<0>{}); hi(); phM(IC<1>{}); lo();
    waitvm<8>();
    STA;
    bar();
    STA;
    phE(dma8(23)); phA(IC<0>{}); phR(none); phF(IC<1>{}); sstore(IC<0>{});
    waitvm<10>();
    STA;
    bar();
    STA;
    head(IC<1>{}, dmah(25));
    waitvm<8>();
    publish(0);
    phA(IC<4>{}); phR(none); phF(IC<0>{}); hi(); phM(IC<1>{}); lo();
    STA;
    bar();
    STA;
    phE(dma8(27)); phA(IC<0>{}); phR(none); phF(IC<1>{});
    sstore(IC<1>{});
    waitvm<2>();
    STA;
    bar();
    STA;
    head(IC<2>{}, [&](auto K) { CI(k, K); if constexpr (k >= 6) issue1(29, k - 6); });
    signal(8);
    phA(IC<4>{}); phR(dma(30));
    waitvm<8>();
    publish(1);
    phF(IC<0>{}); phM(IC<1>{});
    await(10);
    STA;
    phE(dma(31)); phA(IC<0>{}); phR(dma(32)); phF(IC<1>{}); sstore(IC<2>{});
    waitvm<10>();
    hi();
    STA;
    bar();
    STA;
    head(IC<3>{}, none);
    hi();
    waitvm<2>();
    if (lane == 0) lds_inc(misc + 4 * 4);
    phA(IC<4>{}); phR(none); phF(IC<0>{}); phM(IC<1>{}); phE(none); phA(IC<0>{}); phR(none); phF(IC<1>{});
    waitvm<0>();
    publish(2);
    sstore(IC<3>{});
    STA;
    waitvm<0>();
    publish(3);
    STA;
    flagck(2, ~tag, tag);
    STA;
    sload(IC<2>{});
    flagck(3, ~tag, tag);
    STA;
    await(9);
    sfor<0, 8>([&](auto I) { swrite1(IC<2>{}, I); sload1(IC<3>{}, I); sfence(); });
    signal(6);
    STA;
    if (j == 0 && threadIdx.x == 256) __hip_atomic_store(epoch, tag, RLX_AGENT);
    sfor<0, 8>([&](auto I) { swrite1(IC<3>{}, I); });
    signal(7);
    STA;
    STDUMP();
  }
}

extern "C" void kernel_launch(void* const* d_in, const int* in_sizes, int n_in, void* d_out, int out_size, void* d_ws,
                              size_t ws_size, hipStream_t stream) {
  (void)hipFuncSetAttribute((const void*)conv_module_kernel, hipFuncAttributeMaxDynamicSharedMemorySize, LDS_BYTES);
  conv_module_kernel<<<256, 512, LDS_BYTES, stream>>>((const char*)d_in[0], (const char*)d_in[1], (const char*)d_in[2],
                                                      (const char*)d_in[3], (char*)d_out, (char*)d_ws);
}
```

```cpp
#define AMPI 1
#define AMPJ 0
#include <hip/hip_runtime.h>
#include <hip/hip_bf16.h>

typedef short s16x4 __attribute__((ext_vector_type(4)));
typedef __bf16 bf16x8 __attribute__((ext_vector_type(8)));
typedef __bf16 bf16x2 __attribute__((ext_vector_type(2)));
typedef float f32x4 __attribute__((ext_vector_type(4)));
typedef float f32x2 __attribute__((ext_vector_type(2)));
typedef unsigned u32x4 __attribute__((ext_vector_type(4)));
typedef unsigned u32x2 __attribute__((ext_vector_type(2)));
typedef unsigned long long u64;
#define LAS __attribute__((address_space(3)))
#define GAS __attribute__((address_space(1)))
#define DEVI __device__ __forceinline__
#define AI __attribute__((always_inline))
#define RLX_AGENT __ATOMIC_RELAXED, __HIP_MEMORY_SCOPE_AGENT

constexpr int T_ = 4000, TILE = 250;
constexpr int SLOT = 16896;
constexpr int NS = 7;
constexpr int HT_OFF = NS * SLOT;
constexpr int HS = 528;
constexpr int HREG = 16 * HS;
constexpr int TAB_OFF = HT_OFF + 64 * HS;
constexpr int TS = 176;
constexpr int MISC_OFF = TAB_OFF + 64 * TS;
constexpr int LDS_BYTES = 160 * 1024;
static_assert(MISC_OFF + 512 <= LDS_BYTES && 64 * HS >= 2 * SLOT);
constexpr size_t WS_FLAG = 8u << 20;
constexpr size_t WS_EPOCH = WS_FLAG + 32 * 4 * 8 * 128;
constexpr size_t WS_STAMP = WS_EPOCH + 32 * 128;
constexpr unsigned CODE_LINES = 208;
constexpr int PF = 3;

#define KROT gi
#define WA warmx(0, 2, 0, 4);
#define WB warmx(0, 2, 4, 4); warmx(64, 2, 0, 8);
#define WBN 2
#define WC warmx(128, 2, 0, 8);
#define WCN 1
#define WD warmx(192, 3, 0, 8);
#define WDN 1
#define WE warmv();
#define WEN 1
#define PSL 2
#define FST 1
#define CST
template <int N> DEVI void waitvm() { asm volatile("s_waitcnt vmcnt(%0)" ::"n"(N) : "memory"); }
template <int N> DEVI void waitlgkm() { asm volatile("s_waitcnt lgkmcnt(%0)" ::"n"(N) : "memory"); }
DEVI void bar() { asm volatile("" ::: "memory"); __builtin_amdgcn_s_barrier(); asm volatile("" ::: "memory"); }
DEVI void sfence() { __builtin_amdgcn_sched_barrier(0); }
DEVI unsigned pk2(float lo, float hi) { f32x2 p = {lo, hi}; return __builtin_bit_cast(unsigned, __builtin_convertvector(p, bf16x2)); }
DEVI bf16x8 ldsA(LAS char* p) { return *(LAS bf16x8*)p; }
DEVI s16x4 ld64(LAS char* p) { return *(LAS s16x4*)p; }
DEVI s16x4 ldtr(LAS char* p) { return __builtin_amdgcn_ds_read_tr16_b64_v4i16((LAS s16x4*)p); }
DEVI bf16x8 cat8(s16x4 a, s16x4 b) { return __builtin_bit_cast(bf16x8, __builtin_shufflevector(a, b, 0, 1, 2, 3, 4, 5, 6, 7)); }
DEVI void mf(f32x4& c, const bf16x8& a, const bf16x8& b) { asm volatile("v_mfma_f32_16x16x32_bf16 %0, %1, %2, %0" : "+v"(c) : "v"(a), "v"(b)); }
DEVI void mf0(f32x4& c, const bf16x8& a, const bf16x8& b) { asm volatile("v_mfma_f32_16x16x32_bf16 %0, %1, %2, 0" : "=&v"(c) : "v"(a), "v"(b)); }
DEVI float vnl2e(float a) { float r; asm volatile("v_mul_f32 %0, 0xbfb8aa3b, %1" : "=v"(r) : "v"(a)); return r; }
DEVI float vadd1(float a) { float r; asm volatile("v_add_f32 %0, 1.0, %1" : "=v"(r) : "v"(a)); return r; }
DEVI float vmul(float a, float b) { float r; asm volatile("v_mul_f32 %0, %1, %2" : "=v"(r) : "v"(a), "v"(b)); return r; }
DEVI float vexp2(float a) { float r; asm volatile("v_exp_f32 %0, %1" : "=v"(r) : "v"(a)); return r; }
DEVI float vrcp(float a) { float r; asm volatile("v_rcp_f32 %0, %1" : "=v"(r) : "v"(a)); return r; }
DEVI unsigned vpk(float a, float b) { unsigned r; asm volatile("v_cvt_pk_bf16_f32 %0, %1, %2" : "=v"(r) : "v"(a), "v"(b)); return r; }
DEVI LAS char* opaque(LAS char* p) { asm volatile("" : "+v"(p)); return p; }
DEVI void lds_inc(unsigned addr) { asm volatile("ds_add_u32 %0, %1" ::"v"(addr), "v"(1u) : "memory"); }
DEVI unsigned lds_inc_rtn(unsigned addr) { unsigned r; asm volatile("ds_add_rtn_u32 %0, %1, %2\n\ts_waitcnt lgkmcnt(0)" : "=&v"(r) : "v"(addr), "v"(1u) : "memory"); return r; }
DEVI unsigned umin_(unsigned a, unsigned b) { return a < b ? a : b; }
DEVI unsigned umax_(unsigned a, unsigned b) { return a > b ? a : b; }
template <int N> DEVI void nops() { asm volatile("s_nop %0" ::"n"(N)); }

template <int V> struct IC { static constexpr int value = V; };
template <int B, int E, class F> DEVI void sfor(F&& f) {
  if constexpr (B < E) { f(IC<B>{}); sfor<B + 1, E>(f); }
}
#define CI(name, arg) constexpr int name = decltype(arg)::value
template <int NB> struct BaseN {
  LAS char* p[NB];
  DEVI BaseN(LAS char* b) {
    p[0] = b;
#pragma unroll
    for (int i = 1; i < NB; i++) p[i] = opaque(b + i * 61440);
  }
  template <int OFF> DEVI LAS char* at() const {
    constexpr int k = OFF / 61440 < NB ? OFF / 61440 : NB - 1;
    static_assert(OFF - k * 61440 < 65536);
    return p[k] + (OFF - k * 61440);
  }
};
typedef BaseN<2> Base2;
typedef BaseN<3> Base3;

#define ST(i) do { if constexpr ((i) == AMPI || (i) == AMPJ) { sfence(); amp_t[(i) == AMPI] = __builtin_amdgcn_s_memrealtime(); sfence(); } } while (0)
#define STA do {} while (0)
#define STDUMP() do { if (bid == 100 && wave == 0) { u64 u_ = __builtin_amdgcn_s_memrealtime() + (amp_t[1] - amp_t[0]) * 50; while (__builtin_amdgcn_s_memrealtime() < u_) __builtin_amdgcn_s_sleep(10); } } while (0)

constexpr int wunit(int m) { return m < 4 ? m : m + 2; }
constexpr int slot_of(int n) {
  return n < 7 ? n * SLOT : n < 9 ? HT_OFF + (n - 7) * SLOT : n < 31 ? ((n - 2) % NS) * SLOT : n < 33 ? slot_of(n - 5) : slot_of(25);
}
constexpr int sbuf(int P, int h) { return slot_of(P == 1 ? 29 + h : P == 2 ? 26 + h : h ? 24 : 28); }
constexpr int sfrag(int P, int hf, int ks) { return sbuf(P, ks >> 3) + (hf * 8 + (ks & 7)) * 1024; }
constexpr int PFS_OFF = MISC_OFF + 256;

__global__ __launch_bounds__(512, 2) void conv_module_kernel(const char* __restrict__ x, const char* __restrict__ w1,
                                                             const char* __restrict__ dw, const char* __restrict__ w2,
                                                             char* __restrict__ out, char* __restrict__ ws) {
  extern __shared__ __attribute__((aligned(16))) char smem_[];
  LAS char* smem = (LAS char*)smem_;
  u64 pc0;
  asm volatile("s_getpc_b64 %0" : "=s"(pc0));
  const int bid = blockIdx.x;
  const int gi = bid >> 6, j = (bid >> 3) & 7, g = (bid & 7) * 4 + gi;
  const int wave = __builtin_amdgcn_readfirstlane(threadIdx.x >> 6);
  const int lane = threadIdx.x & 63;
  const int batch = g >> 4, t0 = (g & 15) * TILE;
  const int cg = j & 3, rh = j >> 2;
  const int q = lane >> 4, l15 = lane & 15;
  const unsigned misc = (unsigned)(size_t)(smem + MISC_OFF);
  GAS u64* epoch = (GAS u64*)(ws + WS_EPOCH + g * 128);
  u64 amp_t[2] = {0, 0};
  ST(0);
  const auto rs = __builtin_amdgcn_make_buffer_rsrc((void*)(ws + (size_t)g * 262144), 0, 262144, 0x00020000);
  GAS u64* flags = (GAS u64*)(ws + WS_FLAG + (size_t)g * 4096);
  const int svo = ((l15 >> 2) * 16 + 2 * j + ((wave >> 1) & 1)) * 1024 + ((2 * (wave & 1) + (q >> 1)) * 16 + 2 * (l15 & 3) + (q & 1)) * 16;

  const int w3 = wave & 3;
  const auto rw2 = __builtin_amdgcn_make_buffer_rsrc((void*)w2, 0, 512 * 1024, 0x00020000);
  const int vv = (4 * w3 + q) * 1024 + cg * 256 + (((l15 >> 2) ^ q) * 64) + (lane & 3) * 16;
  auto issueV = [&](int n, int i) {
    __builtin_amdgcn_raw_ptr_buffer_load_lds(rw2, smem + slot_of(n) + w3 * 1024 + i * 4096, 16, vv, ((n - 26) * 64 + 16 * i) * 1024, 0, 0);
  };
  u32x4 sf[2][8];
  auto sload1 = [&](auto PP, auto I) AI {
    CI(P, PP); CI(i, I);
    sf[P & 1][i] = __builtin_amdgcn_raw_buffer_load_b128(rs, lane * 16 + w3 * 1024, ((4 * P + 2 * rh + ((i & 3) >> 1)) * 16 + 8 * (i >> 2) + 4 * (i & 1)) * 1024, 16);
  };
  auto sload = [&](auto PP) AI { sfor<0, 8>([&](auto I) AI { sload1(PP, I); }); };
  auto flagld = [&](int P) { return __hip_atomic_load(flags + P * 32 + (lane & 7) * FST, RLX_AGENT); };
  auto flagck = [&](int P, u64 v, u64 tag) AI {
    if (!__all(v == tag)) {
      unsigned spins = 0;
#pragma unroll 1
      do { __builtin_amdgcn_s_sleep(PSL); v = flagld(P); } while (!__all(v == tag) && ++spins < (1u << 20));
    }
    asm volatile("" ::: "memory");
  };
  auto signal = [&](int w) {
    waitlgkm<0>();
    if (lane == 0) lds_inc(misc + w * 4);
  };
  auto await = [&](int w, unsigned target = 4) {
    volatile LAS unsigned* words = (volatile LAS unsigned*)(smem + MISC_OFF);
#pragma unroll 1
    for (unsigned spins = 0; spins < (1u << 24); spins++) {
      if ((unsigned)__builtin_amdgcn_readfirstlane(words[w]) == target) break;
      __builtin_amdgcn_s_sleep(1);
    }
    asm volatile("" ::: "memory");
  };

  if (wave < 4) {
    const int ng = wave;
    __builtin_amdgcn_s_setprio(2);
    if (ng == 1) {
      const auto rc = __builtin_amdgcn_make_buffer_rsrc((void*)pc0, 0, CODE_LINES * 128, 0x00020000);
      __builtin_amdgcn_raw_ptr_buffer_load_lds(rc, smem + PFS_OFF, 4, ((bid >> 3) * 8 + (lane & 7)) * 128, 0, 0, 0);
    }
    const auto rout = __builtin_amdgcn_make_buffer_rsrc((void*)(out + (size_t)(batch * T_ + t0) * 1024), 0, TILE * 1024, 0x00020000);
    const u64 tag = __hip_atomic_load(epoch, RLX_AGENT) + 0x9E3779B97F4A7C15ull;
    {
      const auto rdw = __builtin_amdgcn_make_buffer_rsrc((void*)dw, 0, 31 * 1024, 0x00020000);
      unsigned tap[11];
      const int vo = (10 * ng - 4) * 1024 + (64 * j + lane) * 2;
#pragma unroll
      for (int m = 0; m < 11; m++) {
        int o = vo + m * 1024;
        asm volatile("" : "+v"(o));
        tap[m] = __builtin_amdgcn_raw_buffer_load_b16(rdw, o, 0, 0);
      }
      LAS unsigned* tb = (LAS unsigned*)(smem + TAB_OFF + lane * TS);
#pragma unroll
      for (int m = 0; m < 10; m++) tb[5 * ng + (m & 1) * 20 + (m >> 1)] = tap[m] | (tap[m + 1] << 16);
      tb[40 + ng] = 0u;
      if (threadIdx.x < 16) *(LAS unsigned*)(smem + MISC_OFF + threadIdx.x * 4) = 0u;
      if (threadIdx.x == 0) *(LAS u64*)(smem + MISC_OFF + 64) = tag;
    }
    const Base2 abase(smem + (l15 >> 1) * 1024 + (l15 >> 2) * 64 + (l15 & 1) * 512 + ((q ^ (l15 & 3)) * 16));
    constexpr int RH = 8 * 1024 + 4 * 64;
    const int q4 = l15 >> 2, p4 = lane & 3;
    const Base3 wa(smem + (8 * q + q4) * 256 + ((ng ^ q4 ^ ((q & 1) << 2)) * 32) + p4 * 8);
    const Base3 wg(smem + (8 * q + q4) * 256 + (((4 + ng) ^ q4 ^ ((q & 1) << 2)) * 32) + p4 * 8);
    LAS char* hbase = opaque(smem + HT_OFF + (ng * 16 + l15) * HS + q * 16);
    LAS char* hwrap = opaque(hbase - (q > 0 ? 256 : 0));
    bf16x8 Ba[16], Bg[16];
    bf16x8 A[PF][2];
    f32x4 ca[3][2], cgt[3][2];
    float e[8];
    u32x4 hp;

    auto loadW = [&](auto R) {
      CI(r, R);
      constexpr int ks = r >> 1, off = slot_of(wunit(ks >> 1)) + (ks & 1) * 8192;
      if constexpr (r & 1) Bg[ks] = cat8(ldtr(wg.at<off>()), ldtr(wg.at<off + 1024>()));
      else Ba[ks] = cat8(ldtr(wa.at<off>()), ldtr(wa.at<off + 1024>()));
    };
    auto glu = [&](auto U, auto PP, auto BP) {
      CI(u0, U); CI(pp, PP); CI(bp, BP);
      constexpr int u = u0 == 0 ? 0 : u0 == 1 ? -1 : u0 - 1;
      if constexpr (u >= 0 && u < 4) { e[2 * u] = vnl2e(cgt[pp][u >> 1][(2 * u) & 3]); e[2 * u + 1] = vnl2e(cgt[pp][u >> 1][(2 * u + 1) & 3]); }
      if constexpr (u >= 4 && u < 12) e[u - 4] = vexp2(e[u - 4]);
      if constexpr (u >= 12 && u < 16) { e[2 * u - 24] = vadd1(e[2 * u - 24]); e[2 * u - 23] = vadd1(e[2 * u - 23]); }
      if constexpr (u >= 16 && u < 24) e[u - 16] = vrcp(e[u - 16]);
      if constexpr (u >= 24 && u < 28) {
        constexpr int i = 2 * u - 48;
        e[i] = vmul(e[i], ca[pp][i >> 2][i & 3]); e[i + 1] = vmul(e[i + 1], ca[pp][i >> 2][(i & 3) + 1]);
      }
      if constexpr (u == 28 || u == 29) { constexpr int i = 2 * u - 56; hp[i] = vpk(e[2 * i], e[2 * i + 1]); hp[i + 1] = vpk(e[2 * i + 2], e[2 * i + 3]); }
      constexpr int t = (bp * 32) & 127;
      if constexpr (u == 30) *(LAS u32x4*)(hbase + t * 2) = hp;
      if constexpr (u == 32) {
        if constexpr (t == 64) {
          *(LAS u32x2*)(hwrap + 256 + 248) = (u32x2){hp[0], hp[1]};
          *(LAS u32x2*)(hbase + 256) = (u32x2){hp[2], hp[3]};
        } else {
          *(LAS u32x2*)(hbase + 256 + ((t + 60) & 127) * 2) = (u32x2){hp[0], hp[1]};
          *(LAS u32x2*)(hbase + 256 + ((t + 60) & 127) * 2 + 8) = (u32x2){hp[2], hp[3]};
        }
      }
    };
    auto step = [&](auto I, auto PP, auto KS, auto FIRST, auto NOFF, auto&& f0, auto&& f1) {
      CI(i, I); CI(pp, PP); CI(ks, KS); CI(first, FIRST); CI(noff, NOFF);
      if constexpr (first) mf0(ca[pp][0], A[i][0], Ba[ks]); else mf(ca[pp][0], A[i][0], Ba[ks]);
      sfence(); f0(); sfence();
      if constexpr (first) mf0(cgt[pp][0], A[i][0], Bg[ks]); else mf(cgt[pp][0], A[i][0], Bg[ks]);
      sfence();
      if constexpr (noff >= 0) A[i][0] = ldsA(abase.at<noff>());
      sfence();
      if constexpr (first) mf0(cgt[pp][1], A[i][1], Bg[ks]); else mf(cgt[pp][1], A[i][1], Bg[ks]);
      sfence(); f1(); sfence();
      if constexpr (first) mf0(ca[pp][1], A[i][1], Ba[ks]); else mf(ca[pp][1], A[i][1], Ba[ks]);
      sfence();
      if constexpr (noff >= 0) A[i][1] = ldsA(abase.at<noff + RH>());
      sfence();
    };
    auto nop = [] {};
    auto primeA = [&](auto N) {
      CI(n, N);
      sfor<0, PF>([&](auto I) { CI(i, I); A[i][0] = ldsA(abase.at<slot_of(n) + i * 64>()); A[i][1] = ldsA(abase.at<slot_of(n) + i * 64 + RH>()); });
    };

    waitlgkm<0>();
    bar();
    bar();
    ST(1);
    sfor<0, 4>(loadW);
    primeA(IC<4>{});
    sfor<0, 16>([&](auto S) {
      CI(s, S);
      constexpr int n = s + PF;
      step(IC<s % PF>{}, IC<(s >> 3)>{}, IC<(s & 7)>{}, IC<(s & 7) == 0>{}, IC<(n < 16 ? slot_of(4 + (n >> 3)) + (n & 7) * 64 : -1)>{},
           [&] { if constexpr (s < 6) loadW(IC<2 * s + 4>{}); },
           [&] { if constexpr (s < 6) loadW(IC<2 * s + 5>{}); });
    });
    ST(2);
    bar();
    bar();
    ST(3);
    sfor<16, 20>(loadW);
    primeA(IC<10>{});
    sfor<0, 16>([&](auto S) {
      CI(s, S);
      constexpr int n = s + PF;
      if constexpr (s == 8 - PF || s == 16 - PF) bar();
      step(IC<s % PF>{}, IC<(s >> 3)>{}, IC<8 + (s & 7)>{}, IC<0>{}, IC<slot_of(10 + (n >> 3)) + (n & 7) * 64>{},
           [&] { if constexpr (s < 6) loadW(IC<2 * s + 20>{});
                 if constexpr (s == 8) nops<7>();
                 if constexpr (s >= 8) { glu(IC<4 * (s - 8)>{}, IC<0>{}, IC<0>{}); glu(IC<4 * (s - 8) + 1>{}, IC<0>{}, IC<0>{}); } },
           [&] { if constexpr (s < 6) loadW(IC<2 * s + 21>{});
                 if constexpr (s >= 8) { glu(IC<4 * (s - 8) + 2>{}, IC<0>{}, IC<0>{}); glu(IC<4 * (s - 8) + 3>{}, IC<0>{}, IC<0>{}); } });
    });
    ST(5);
    sfor<2, 7>([&](auto BB) {
      CI(b, BB);
      sfor<0, 16>([&](auto S) {
        CI(s, S);
        constexpr int n = s + PF, gs = 16 * (b - 1) + s;
        if constexpr (s == 3) { waitlgkm<(b & 3) == 0 ? 4 : 4>(); bar(); }
        CST
        auto fill = [&](auto G) {
          CI(gp, G);
          if constexpr (b > 2 || gp > 0) glu(IC<gp + 31>{}, IC<(b & 1)>{}, IC<b - 2>{});
          glu(IC<gp - 1>{}, IC<(b & 1) ^ 1>{}, IC<b - 1>{});
        };
        step(IC<gs % PF>{}, IC<(b & 1)>{}, IC<s>{}, IC<s == 0>{}, IC<slot_of(8 + 2 * b + (n >> 3)) + (n & 7) * 64>{},
             [&] { fill(IC<2 * s>{}); }, [&] { fill(IC<2 * s + 1>{}); });
      });
    });
    const Base2 vb(smem + (8 * q + q4) * 256 + ((ng ^ q4) * 64) + p4 * 16);
    auto loadV = [&](auto R) {
      CI(r, R);
      constexpr int ks = r >> 1, off = slot_of(26 + (ks >> 1)) + (ks & 1) * 8192 + (r & 1) * 8;
      if constexpr (r & 1) Bg[ks] = cat8(ldtr(vb.at<off>()), ldtr(vb.at<off + 1024>()));
      else Ba[ks] = cat8(ldtr(vb.at<off>()), ldtr(vb.at<off + 1024>()));
    };
    sfor<0, 32>([&](auto S) {
      CI(s, S);
      constexpr int n = s + PF, gs = 96 + s, set = 1 + ((s >> 3) & 1), ks = (s >> 4) * 8 + (s & 7);
      if constexpr (s == 3 || s == 19) { waitlgkm<4>(); bar(); }
      if constexpr (s == 27) { waitlgkm<5>(); if (lane == 0) lds_inc(misc + 10 * 4); }
      auto fill = [&](auto G) {
        CI(gp, G);
        glu(IC<gp + 31>{}, IC<1>{}, IC<5>{});
        glu(IC<gp - 1>{}, IC<0>{}, IC<6>{});
        if constexpr (gp >= 38 && gp < 50) loadV(IC<gp - 38>{});
        if constexpr (gp >= 50) glu(IC<gp - 50>{}, IC<1>{}, IC<7>{});
      };
      step(IC<gs % PF>{}, IC<set>{}, IC<ks>{}, IC<ks == 0>{}, IC<(n < 32 ? slot_of(22 + (n >> 3)) + (n & 7) * 64 : -1)>{},
           [&] { fill(IC<2 * s>{}); }, [&] { fill(IC<2 * s + 1>{}); });
    });
    ST(6);
    u64 fv = flagld(0);
    sfor<14, 32>([&](auto U) { glu(U, IC<1>{}, IC<7>{}); });
    volatile LAS unsigned* words = (volatile LAS unsigned*)(smem + MISC_OFF);
    unsigned fl = words[8];
    sfor<0, 31>([&](auto U) { CI(u, U); glu(IC<u + 32>{}, IC<1>{}, IC<7>{}); glu(U, IC<2>{}, IC<8>{}); });
    if (__builtin_amdgcn_readfirstlane(fl) != 4) await(8);
    asm volatile("" ::: "memory");
    sfor<31, 34>([&](auto U) { glu(U, IC<2>{}, IC<8>{}); });
    waitlgkm<0>();
    bar();
    ST(7);
    const Base2 sbase(smem + lane * 16);
    const int tt = 4 * ((l15 >> 1) & 3) + 2 * (l15 >> 3) + (l15 & 1);
    const int ovo = (rh * 32 + tt) * 1024 + cg * 256 + ng * 64 + q * 16;
    unsigned op[4];
    auto ostep = [&](auto U, auto PP, auto PC) {
      CI(u, U); CI(pp, PP); CI(pc, PC);
      constexpr int hf = u / 3, s = u % 3;
      if constexpr (u < 6) {
        if constexpr (s == 0) { op[0] = vpk(ca[pp][hf][0], ca[pp][hf][1]); op[1] = vpk(ca[pp][hf][2], ca[pp][hf][3]); }
        if constexpr (s == 1) { op[2] = vpk(cgt[pp][hf][0], cgt[pp][hf][1]); op[3] = vpk(cgt[pp][hf][2], cgt[pp][hf][3]); }
        if constexpr (s == 2) __builtin_amdgcn_raw_buffer_store_b128((u32x4){op[0], op[1], op[2], op[3]}, rout, ovo + (pc * 64 + hf * 16) * 1024, 0, 0);
      }
    };
    __builtin_amdgcn_s_setprio(0);
    const Base2 swb(smem + ng * 1024 + lane * 16);
    auto swrite1 = [&](auto PP, auto I) { CI(P, PP); CI(i, I); *(LAS u32x4*)(swb.at<sbuf(P, i >> 2) + (i & 3) * 4096>()) = sf[P & 1][i]; };
    flagck(0, fv, tag);
    sload(IC<0>{});
    fv = flagld(1);
    sfor<12, 20>(loadV);
    sfence();
    sfor<0, 4>([&](auto I) { CI(i, I); issueV(33, i); });
    flagck(1, fv, tag);
    ST(14);
    sfor<0, 8>([&](auto I) { swrite1(IC<0>{}, I); sload1(IC<1>{}, I); sfence(); });
    waitvm<8>();
    signal(4);
    await(4, 8);
    ST(8);
    sfor<0, PF>([&](auto I) { CI(i, I); A[i][0] = ldsA(sbase.at<sfrag(0, 0, i)>()); A[i][1] = ldsA(sbase.at<sfrag(0, 1, i)>()); });
    sfor<0, 64>([&](auto S) {
      CI(gs, S);
      constexpr int P = gs >> 4, s = gs & 15, i = gs % PF, pp = P & 1, n = gs + PF;
      constexpr int idx = (n == 16 || n == 32 || n == 48) ? (n >> 4) : -1;
      constexpr int n2 = n + 1, idx2 = (n2 == 16 || n2 == 32 || n2 == 48) ? (n2 >> 4) : -1;
      if constexpr (idx >= 0) {
        if (__builtin_amdgcn_readfirstlane(fl) != 4) await(4 + idx);
        asm volatile("" ::: "memory");
        if constexpr (idx > 0) ST(8 + idx);
      }
      if constexpr (s == 0) mf0(ca[pp][0], Ba[s], A[i][0]); else mf(ca[pp][0], Ba[s], A[i][0]);
      sfence();
      if constexpr (P > 0 && s >= 2) ostep(IC<s - 2>{}, IC<pp ^ 1>{}, IC<P - 1>{});
      if constexpr (gs < 6) loadV(IC<20 + 2 * gs>{});
      if constexpr (gs >= 6 && gs < 10) swrite1(IC<1>{}, IC<2 * gs - 12>{});
      if constexpr (idx2 >= 0) fl = words[4 + idx2];
      sfence();
      if constexpr (s == 0) mf0(cgt[pp][0], Bg[s], A[i][0]); else mf(cgt[pp][0], Bg[s], A[i][0]);
      sfence();
      if constexpr (n < 64) A[i][0] = ldsA(sbase.at<sfrag((n & 63) >> 4, 0, n & 15)>());
      sfence();
      if constexpr (s == 0) mf0(cgt[pp][1], Bg[s], A[i][1]); else mf(cgt[pp][1], Bg[s], A[i][1]);
      sfence();
      if constexpr (gs < 6) loadV(IC<21 + 2 * gs>{});
      if constexpr (gs >= 6 && gs < 10) swrite1(IC<1>{}, IC<2 * gs - 11>{});
      if constexpr (gs == 11) { waitlgkm<4>(); if (lane == 0) lds_inc(misc + 5 * 4); }
      if constexpr (gs == 17) { if (lane == 0) lds_inc(misc + 9 * 4); }
      sfence();
      if constexpr (s == 0) mf0(ca[pp][1], Ba[s], A[i][1]); else mf(ca[pp][1], Ba[s], A[i][1]);
      sfence();
      if constexpr (n < 64) A[i][1] = ldsA(sbase.at<sfrag((n & 63) >> 4, 1, n & 15)>());
      sfence();
    });
    ST(12);
    nops<7>();
    sfor<0, 6>([&](auto U) { ostep(U, IC<1>{}, IC<3>{}); nops<1>(); });
    ST(13);
    STDUMP();
  } else {
    const int hw = wave - 4;
    const auto rx = __builtin_amdgcn_make_buffer_rsrc((void*)(x + (size_t)batch * (T_ * 1024)), 0, T_ * 1024, 0x00020000);
    const auto rw1 = __builtin_amdgcn_make_buffer_rsrc((void*)w1, 0, 1024 * 1024, 0x00020000);
    const int lc = (l15 >> 1) ^ q ^ ((hw >> 1) << 2);
    const int vw = (4 * hw + q) * 2048 + (lc >> 2) * 1024 + j * 128 + (lc & 3) * 32 + (lane & 1) * 16;
    const int vx = (t0 - 15 + 8 * (hw >> 1) + 2 * (hw & 1) + (lane >> 5)) * 1024 + (((((lane & 31) ^ (2 * (hw & 1) + (lane >> 5))) * 16) + KROT * 128) & 511);
    const int vx1 = vx + 32768;
    auto issue1 = [&](int n, int i) {
      if (n < 4 || (n >= 6 && n < 10)) {
        const int m = n < 4 ? n : n - 2;
        __builtin_amdgcn_raw_ptr_buffer_load_lds(rw1, smem + slot_of(n) + hw * 1024 + i * 4096, 16, vw, (((m & 4) + ((m + KROT) & 3)) * 64 + 16 * i) * 2048, 0, 0);
      } else if (n < 26) {
        const int b = n == 4 || n == 10 ? 0 : n == 5 || n == 11 ? 1 : n < 22 ? (n - 8) >> 1 : 7 + (n & 1);
        const int half = n < 6 ? 0 : n < 12 ? 1 : n < 22 ? (n & 1) : (n - 22) >> 1;
        LAS char* dst = smem + slot_of(n) + hw * 1024 + (hw >> 1) * 64 + i * (4096 + 128);
        if (b == 0) __builtin_amdgcn_raw_ptr_buffer_load_lds(rx, dst, 16, vx + half * 512 + (16 * (i & 1) + 4 * (i >> 1)) * 1024, 0, 0, 0);
        else __builtin_amdgcn_raw_ptr_buffer_load_lds(rx, dst, 16, vx1, (b - 1) * 32768 + half * 512 + (16 * (i & 1) + 4 * (i >> 1)) * 1024, 0, 0);
      } else {
        issueV(n, i);
      }
    };
    auto issue = [&](int n) {
#pragma unroll
      for (int i = 0; i < 4; i++) issue1(n, i);
    };
    auto warmx = [&](int r0, int rpw, int l0, int nl) {
      const int r = lane / nl;
      const int o = r < rpw ? (t0 - 15 + r0 + (4 * j + hw) * rpw + r) * 1024 + (l0 + lane % nl) * 128 : -1;
      __builtin_amdgcn_raw_ptr_buffer_load_lds(rx, smem + PFS_OFF, 4, o, 0, 0, 0);
    };
    auto warmv = [&] {
      __builtin_amdgcn_raw_ptr_buffer_load_lds(rw2, smem + PFS_OFF, 4, lane < 32 ? (((bid >> 3) * 4 + hw) * 32 + lane) * 128 : -1, 0, 0, 0);
    };
    STA;
    issue(0);
    WA
    issue(1); issue(2); issue(3); issue(4); issue(5); issue(6);
    WB
    bar();
    const u64 tag = *(LAS u64*)(smem + MISC_OFF + 64);
    bf16x8 T1[16], T2[4];
    {
      const int e4 = l15 >> 2, b3 = lane & 3;
      LAS char* tb = smem + TAB_OFF + 16 * hw * TS;
      const int i1 = (b3 & 1) * 20 + ((8 * q - b3 + 4) >> 1), i2 = (b3 & 1) * 20 + ((36 - b3) >> 1);
      sfor<0, 4>([&](auto EE) {
        CI(ee, EE);
        LAS unsigned* p = (LAS unsigned*)opaque(tb + 4 * ee * TS + (e4 == ee ? i1 : 40) * 4);
        sfor<0, 4>([&](auto QD) {
          CI(qd, QD);
          const u32x4 v = {p[qd * 44], p[qd * 44 + 1], p[qd * 44 + 2], p[qd * 44 + 3]};
          sfence();
          T1[4 * ee + qd] = __builtin_bit_cast(bf16x8, v);
        });
      });
      LAS unsigned* p = (LAS unsigned*)opaque(tb + 4 * q * TS + (e4 == q ? i2 : 40) * 4);
      sfor<0, 4>([&](auto QD) { CI(qd, QD); const u32x4 v = {0u, 0u, p[qd * 44], p[qd * 44 + 1]}; sfence(); T2[qd] = __builtin_bit_cast(bf16x8, v); });
    }
    LAS char* c1[2]; LAS char* c2[2];
    sfor<0, 2>([&](auto PP) {
      CI(pp, PP);
      const int sh = (l15 & 1) * 60 + 64 * pp + 4 * l15;
      c1[pp] = opaque(smem + HT_OFF + hw * HREG + (l15 & 1) * 256 + ((sh + 8 * q) & 127) * 2);
      const int op = (l15 & 1) ^ 1;
      c2[pp] = opaque(smem + HT_OFF + hw * HREG + 4 * q * HS + op * 256 + ((op * 60 + 64 * pp + 4 * l15 + 28) & 127) * 2);
    });
    waitvm<4 + WBN>();
    STA;
    bar();
    STA;
    issue(7); issue(8);
    STA;
    bar();
    STA;
    issue(9); issue(10); issue(11); issue(12);
    waitvm<8>();
    STA;
    bar();
    STA;
    issue(13);
    waitvm<8>();
    STA;
    bar();
    STA;
    issue(14);
    WC
    waitvm<8 + WCN>();
    STA;
    bar();
    STA;
    issue(15); issue(16);
    waitvm<8 + WCN>();
    STA;
    bar();
    STA;
    issue(17); issue(18);
    WD
    waitvm<8 + WDN>();
    STA;
    bar();
    STA;
    issue(19); issue(20);
    WE
    f32x4 d[4];
    unsigned sp[4][2];
    bf16x8 W[20];
    float sv[8], stt[8];
    auto convrd1 = [&](auto PP, auto K) {
      CI(P, PP); CI(k, K);
      constexpr int hh = k / 10, ee = (k % 10) >> 1, qq = k & 1, qd = 2 * hh + qq;
      W[10 * hh + 5 * qq + ee] = ldsA(ee < 4 ? c1[P & 1] + (4 * ee + qd) * HS : c2[P & 1] + qd * HS);
      sfence();
    };
    auto lo = [] { sfence(); __builtin_amdgcn_s_setprio(0); sfence(); };
    auto hi = [] { sfence(); __builtin_amdgcn_s_setprio(3); sfence(); };
    auto convmm = [&](auto HH, auto&& f) {
      CI(hh, HH);
      hi();
      sfor<0, 10>([&](auto K) {
        CI(k, K);
        constexpr int ee = k >> 1, qq = k & 1, qd = 2 * hh + qq;
        const bf16x8& t = ee < 4 ? T1[(4 * ee + qd) & 15] : T2[qd];
        d[qd] = __builtin_amdgcn_mfma_f32_16x16x32_bf16(t, W[10 * hh + 5 * qq + ee], ee == 0 ? (f32x4){0.f, 0.f, 0.f, 0.f} : d[qd], 0, 0, 0);
        sfence(); f(K); sfence();
      });
      sfence();
    };
    auto none = [](auto) {};
    auto shadowEA = [&](auto K) { CI(k, K); if constexpr (k < 8) stt[k] = vexp2(stt[k]); else { stt[2 * k - 16] = vadd1(stt[2 * k - 16]); stt[2 * k - 15] = vadd1(stt[2 * k - 15]); } };
    auto phM = [&](auto HH) { CI(hh, HH); sfor<0, 8>([&](auto I) { CI(i, I); sv[i] = d[2 * hh + (i & 1)][i >> 1]; stt[i] = sv[i] * -1.4426950408889634f; sfence(); }); };
    auto head = [&](auto PP, auto&& g) AI {
      sfor<0, 10>([&](auto K) { convrd1(PP, K); g(K); sfence(); });
      convmm(IC<0>{}, [&](auto K) { CI(k, K); convrd1(PP, IC<10 + k>{}); });
      phM(IC<0>{}); convmm(IC<1>{}, shadowEA); lo();
    };
    auto phE = [&](auto&& g) { sfor<0, 8>([&](auto I) { CI(i, I); stt[i] = vexp2(stt[i]); g(I); sfence(); }); };
    auto phA = [&](auto B0) { CI(b0, B0); sfor<b0, 8>([&](auto I) { CI(i, I); stt[i] = vadd1(stt[i]); }); sfence(); };
    auto phR = [&](auto&& g) { sfor<0, 8>([&](auto I) { CI(i, I); stt[i] = vrcp(stt[i]); g(I); sfence(); }); };
    auto phF = [&](auto HH) {
      CI(hh, HH);
      sfor<0, 8>([&](auto I) { CI(i, I); stt[i] = vmul(stt[i], sv[i]); });
      sfor<0, 4>([&](auto R) { CI(r, R); sp[r][hh] = pk2(stt[2 * r], stt[2 * r + 1]); });
      sfence();
    };
    auto dma = [&](int n) { return [&issue1, n](auto I) { CI(i, I); if constexpr (i & 1) issue1(n, i >> 1); }; };
    auto sstore = [&](auto PP) {
      CI(P, PP);
      sfor<0, 2>([&](auto I) {
        CI(i, I);
        auto r0 = __builtin_amdgcn_permlane16_swap(sp[2 * i][0], sp[2 * i + 1][0], false, false);
        auto r1 = __builtin_amdgcn_permlane16_swap(sp[2 * i][1], sp[2 * i + 1][1], false, false);
        __builtin_amdgcn_raw_buffer_store_b128((u32x4){r0[0], r1[0], r0[1], r1[1]}, rs, svo + P * 65536 + i * 128, 0, 16);
      });
    };
    const Base2 swb(smem + hw * 1024 + lane * 16);
    auto swrite1 = [&](auto PP, auto I) { CI(P, PP); CI(i, I); *(LAS u32x4*)(swb.at<sbuf(P, i >> 2) + (i & 3) * 4096>()) = sf[P & 1][i]; };
    auto dma8 = [&](int n) { return [&issue1, n](auto I) { CI(i, I); issue1(n + (i >> 2), i & 3); }; };
    auto dma3 = [&](int n) { return [&issue1, n](auto K) { CI(k, K); if constexpr (k == 1 || k == 4 || k == 7) issue1(n, 1 + k / 3); }; };
    auto flagst = [&](int P) { if (threadIdx.x == 256) __hip_atomic_store(flags + P * 32 + j * FST, tag, RLX_AGENT); };
    auto publish = [&](int P) {
      if (lane == 0) {
        if (lds_inc_rtn(misc + P * 4) == 3) __hip_atomic_store(flags + P * 32 + j * FST, tag, RLX_AGENT);
      }
    };
    auto dmah = [&](int n) { return [&issue1, n](auto K) { CI(k, K); if constexpr (k < 8) issue1(n + (k >> 2), k & 3); }; };
    waitvm<8 + WDN + WEN>();
    STA;
    bar();
    STA;
    head(IC<0>{}, dmah(21));
    phA(IC<4>{}); phR(none); phF(IC<0>{}); hi(); phM(IC<1>{}); lo();
    waitvm<8>();
    STA;
    bar();
    STA;
    phE(dma8(23)); phA(IC<0>{}); phR(none); phF(IC<1>{}); sstore(IC<0>{});
    waitvm<10>();
    STA;
    bar();
    STA;
    head(IC<1>{}, dmah(25));
    waitvm<8>();
    publish(0);
    phA(IC<4>{}); phR(none); phF(IC<0>{}); hi(); phM(IC<1>{}); lo();
    STA;
    bar();
    STA;
    phE(dma8(27)); phA(IC<0>{}); phR(none); phF(IC<1>{});
    sstore(IC<1>{});
    waitvm<2>();
    STA;
    bar();
    STA;
    head(IC<2>{}, [&](auto K) { CI(k, K); if constexpr (k >= 6) issue1(29, k - 6); });
    signal(8);
    phA(IC<4>{}); phR(dma(30));
    waitvm<8>();
    publish(1);
    phF(IC<0>{}); phM(IC<1>{});
    await(10);
    STA;
    phE(dma(31)); phA(IC<0>{}); phR(dma(32)); phF(IC<1>{}); sstore(IC<2>{});
    waitvm<10>();
    hi();
    STA;
    bar();
    STA;
    head(IC<3>{}, none);
    hi();
    waitvm<2>();
    if (lane == 0) lds_inc(misc + 4 * 4);
    phA(IC<4>{}); phR(none); phF(IC<0>{}); phM(IC<1>{}); phE(none); phA(IC<0>{}); phR(none); phF(IC<1>{});
    waitvm<0>();
    publish(2);
    sstore(IC<3>{});
    STA;
    waitvm<0>();
    publish(3);
    STA;
    flagck(2, ~tag, tag);
    STA;
    sload(IC<2>{});
    flagck(3, ~tag, tag);
    STA;
    await(9);
    sfor<0, 8>([&](auto I) { swrite1(IC<2>{}, I); sload1(IC<3>{}, I); sfence(); });
    signal(6);
    STA;
    if (j == 0 && threadIdx.x == 256) __hip_atomic_store(epoch, tag, RLX_AGENT);
    sfor<0, 8>([&](auto I) { swrite1(IC<3>{}, I); });
    signal(7);
    STA;
    STDUMP();
  }
}

extern "C" void kernel_launch(void* const* d_in, const int* in_sizes, int n_in, void* d_out, int out_size, void* d_ws,
                              size_t ws_size, hipStream_t stream) {
  (void)hipFuncSetAttribute((const void*)conv_module_kernel, hipFuncAttributeMaxDynamicSharedMemorySize, LDS_BYTES);
  conv_module_kernel<<<256, 512, LDS_BYTES, stream>>>((const char*)d_in[0], (const char*)d_in[1], (const char*)d_in[2],
                                                      (const char*)d_in[3], (char*)d_out, (char*)d_ws);
}
```

```cpp
#define AMPM 0
#include <hip/hip_runtime.h>
#include <hip/hip_bf16.h>

typedef short s16x4 __attribute__((ext_vector_type(4)));
typedef __bf16 bf16x8 __attribute__((ext_vector_type(8)));
typedef __bf16 bf16x2 __attribute__((ext_vector_type(2)));
typedef float f32x4 __attribute__((ext_vector_type(4)));
typedef float f32x2 __attribute__((ext_vector_type(2)));
typedef unsigned u32x4 __attribute__((ext_vector_type(4)));
typedef unsigned u32x2 __attribute__((ext_vector_type(2)));
typedef unsigned long long u64;
#define LAS __attribute__((address_space(3)))
#define GAS __attribute__((address_space(1)))
#define DEVI __device__ __forceinline__
#define AI __attribute__((always_inline))
#define RLX_AGENT __ATOMIC_RELAXED, __HIP_MEMORY_SCOPE_AGENT

constexpr int T_ = 4000, TILE = 250;
constexpr int SLOT = 16896;
constexpr int NS = 7;
constexpr int HT_OFF = NS * SLOT;
constexpr int HS = 528;
constexpr int HREG = 16 * HS;
constexpr int TAB_OFF = HT_OFF + 64 * HS;
constexpr int TS = 176;
constexpr int MISC_OFF = TAB_OFF + 64 * TS;
constexpr int LDS_BYTES = 160 * 1024;
static_assert(MISC_OFF + 512 <= LDS_BYTES && 64 * HS >= 2 * SLOT);
constexpr size_t WS_FLAG = 8u << 20;
constexpr size_t WS_EPOCH = WS_FLAG + 32 * 4 * 8 * 128;
constexpr size_t WS_STAMP = WS_EPOCH + 32 * 128;
constexpr unsigned CODE_LINES = 208;
constexpr int PF = 3;

#define KROT gi
#define WA warmx(0, 2, 0, 4);
#define WB warmx(0, 2, 4, 4); warmx(64, 2, 0, 8);
#define WBN 2
#define WC warmx(128, 2, 0, 8);
#define WCN 1
#define WD warmx(192, 3, 0, 8);
#define WDN 1
#define WE warmv();
#define WEN 1
#define PSL 2
#define FST 1
#define CST
template <int N> DEVI void waitvm() { asm volatile("s_waitcnt vmcnt(%0)" ::"n"(N) : "memory"); }
template <int N> DEVI void waitlgkm() { asm volatile("s_waitcnt lgkmcnt(%0)" ::"n"(N) : "memory"); }
DEVI void bar() { asm volatile("" ::: "memory"); __builtin_amdgcn_s_barrier(); asm volatile("" ::: "memory"); }
DEVI void sfence() { __builtin_amdgcn_sched_barrier(0); }
DEVI unsigned pk2(float lo, float hi) { f32x2 p = {lo, hi}; return __builtin_bit_cast(unsigned, __builtin_convertvector(p, bf16x2)); }
DEVI bf16x8 ldsA(LAS char* p) { return *(LAS bf16x8*)p; }
DEVI s16x4 ld64(LAS char* p) { return *(LAS s16x4*)p; }
DEVI s16x4 ldtr(LAS char* p) { return __builtin_amdgcn_ds_read_tr16_b64_v4i16((LAS s16x4*)p); }
DEVI bf16x8 cat8(s16x4 a, s16x4 b) { return __builtin_bit_cast(bf16x8, __builtin_shufflevector(a, b, 0, 1, 2, 3, 4, 5, 6, 7)); }
DEVI void mf(f32x4& c, const bf16x8& a, const bf16x8& b) { asm volatile("v_mfma_f32_16x16x32_bf16 %0, %1, %2, %0" : "+v"(c) : "v"(a), "v"(b)); }
DEVI void mf0(f32x4& c, const bf16x8& a, const bf16x8& b) { asm volatile("v_mfma_f32_16x16x32_bf16 %0, %1, %2, 0" : "=&v"(c) : "v"(a), "v"(b)); }
DEVI float vnl2e(float a) { float r; asm volatile("v_mul_f32 %0, 0xbfb8aa3b, %1" : "=v"(r) : "v"(a)); return r; }
DEVI float vadd1(float a) { float r; asm volatile("v_add_f32 %0, 1.0, %1" : "=v"(r) : "v"(a)); return r; }
DEVI float vmul(float a, float b) { float r; asm volatile("v_mul_f32 %0, %1, %2" : "=v"(r) : "v"(a), "v"(b)); return r; }
DEVI float vexp2(float a) { float r; asm volatile("v_exp_f32 %0, %1" : "=v"(r) : "v"(a)); return r; }
DEVI float vrcp(float a) { float r; asm volatile("v_rcp_f32 %0, %1" : "=v"(r) : "v"(a)); return r; }
DEVI unsigned vpk(float a, float b) { unsigned r; asm volatile("v_cvt_pk_bf16_f32 %0, %1, %2" : "=v"(r) : "v"(a), "v"(b)); return r; }
DEVI LAS char* opaque(LAS char* p) { asm volatile("" : "+v"(p)); return p; }
DEVI void lds_inc(unsigned addr) { asm volatile("ds_add_u32 %0, %1" ::"v"(addr), "v"(1u) : "memory"); }
DEVI unsigned lds_inc_rtn(unsigned addr) { unsigned r; asm volatile("ds_add_rtn_u32 %0, %1, %2\n\ts_waitcnt lgkmcnt(0)" : "=&v"(r) : "v"(addr), "v"(1u) : "memory"); return r; }
DEVI unsigned umin_(unsigned a, unsigned b) { return a < b ? a : b; }
DEVI unsigned umax_(unsigned a, unsigned b) { return a > b ? a : b; }
template <int N> DEVI void nops() { asm volatile("s_nop %0" ::"n"(N)); }

template <int V> struct IC { static constexpr int value = V; };
template <int B, int E, class F> DEVI void sfor(F&& f) {
  if constexpr (B < E) { f(IC<B>{}); sfor<B + 1, E>(f); }
}
#define CI(name, arg) constexpr int name = decltype(arg)::value
template <int NB> struct BaseN {
  LAS char* p[NB];
  DEVI BaseN(LAS char* b) {
    p[0] = b;
#pragma unroll
    for (int i = 1; i < NB; i++) p[i] = opaque(b + i * 61440);
  }
  template <int OFF> DEVI LAS char* at() const {
    constexpr int k = OFF / 61440 < NB ? OFF / 61440 : NB - 1;
    static_assert(OFF - k * 61440 < 65536);
    return p[k] + (OFF - k * 61440);
  }
};
typedef BaseN<2> Base2;
typedef BaseN<3> Base3;

#define ST(i) do { if constexpr ((i) == 0) { amp_t = __builtin_amdgcn_s_memrealtime(); } } while (0)
#define STA do {} while (0)
#define STDUMP() do { if (wave == 0) { \
    waitvm<0>(); u64 te_ = __builtin_amdgcn_s_memrealtime(); GAS u64* sp_ = (GAS u64*)(ws + WS_STAMP); \
    if (lane == 0) { __hip_atomic_store(sp_ + bid * 2, amp_t, RLX_AGENT); __hip_atomic_store(sp_ + bid * 2 + 1, te_, RLX_AGENT); } \
    if (bid == 100) { while (__builtin_amdgcn_s_memrealtime() < te_ + 5000) __builtin_amdgcn_s_sleep(10); \
      u64 mn_ = ~0ull, mxs_ = 0, mxe_ = 0; \
      for (int i_ = 0; i_ < 256; i_++) { u64 a_ = __hip_atomic_load(sp_ + i_ * 2, RLX_AGENT), b_ = __hip_atomic_load(sp_ + i_ * 2 + 1, RLX_AGENT); mn_ = a_ < mn_ ? a_ : mn_; mxs_ = a_ > mxs_ ? a_ : mxs_; mxe_ = b_ > mxe_ ? b_ : mxe_; } \
      u64 d_ = AMPM == 1 ? mxs_ - mn_ : AMPM == 2 ? mxe_ - mn_ : 0; if (d_ > 10000) d_ = 10000; \
      u64 u_ = te_ + 30000 + d_ * 50; while (__builtin_amdgcn_s_memrealtime() < u_) __builtin_amdgcn_s_sleep(10); } } } while (0)

constexpr int wunit(int m) { return m < 4 ? m : m + 2; }
constexpr int slot_of(int n) {
  return n < 7 ? n * SLOT : n < 9 ? HT_OFF + (n - 7) * SLOT : n < 31 ? ((n - 2) % NS) * SLOT : n < 33 ? slot_of(n - 5) : slot_of(25);
}
constexpr int sbuf(int P, int h) { return slot_of(P == 1 ? 29 + h : P == 2 ? 26 + h : h ? 24 : 28); }
constexpr int sfrag(int P, int hf, int ks) { return sbuf(P, ks >> 3) + (hf * 8 + (ks & 7)) * 1024; }
constexpr int PFS_OFF = MISC_OFF + 256;

__global__ __launch_bounds__(512, 2) void conv_module_kernel(const char* __restrict__ x, const char* __restrict__ w1,
                                                             const char* __restrict__ dw, const char* __restrict__ w2,
                                                             char* __restrict__ out, char* __restrict__ ws) {
  extern __shared__ __attribute__((aligned(16))) char smem_[];
  LAS char* smem = (LAS char*)smem_;
  u64 pc0;
  asm volatile("s_getpc_b64 %0" : "=s"(pc0));
  const int bid = blockIdx.x;
  const int gi = bid >> 6, j = (bid >> 3) & 7, g = (bid & 7) * 4 + gi;
  const int wave = __builtin_amdgcn_readfirstlane(threadIdx.x >> 6);
  const int lane = threadIdx.x & 63;
  const int batch = g >> 4, t0 = (g & 15) * TILE;
  const int cg = j & 3, rh = j >> 2;
  const int q = lane >> 4, l15 = lane & 15;
  const unsigned misc = (unsigned)(size_t)(smem + MISC_OFF);
  GAS u64* epoch = (GAS u64*)(ws + WS_EPOCH + g * 128);
  u64 amp_t = 0;
  ST(0);
  const auto rs = __builtin_amdgcn_make_buffer_rsrc((void*)(ws + (size_t)g * 262144), 0, 262144, 0x00020000);
  GAS u64* flags = (GAS u64*)(ws + WS_FLAG + (size_t)g * 4096);
  const int svo = ((l15 >> 2) * 16 + 2 * j + ((wave >> 1) & 1)) * 1024 + ((2 * (wave & 1) + (q >> 1)) * 16 + 2 * (l15 & 3) + (q & 1)) * 16;

  const int w3 = wave & 3;
  const auto rw2 = __builtin_amdgcn_make_buffer_rsrc((void*)w2, 0, 512 * 1024, 0x00020000);
  const int vv = (4 * w3 + q) * 1024 + cg * 256 + (((l15 >> 2) ^ q) * 64) + (lane & 3) * 16;
  auto issueV = [&](int n, int i) {
    __builtin_amdgcn_raw_ptr_buffer_load_lds(rw2, smem + slot_of(n) + w3 * 1024 + i * 4096, 16, vv, ((n - 26) * 64 + 16 * i) * 1024, 0, 0);
  };
  u32x4 sf[2][8];
  auto sload1 = [&](auto PP, auto I) AI {
    CI(P, PP); CI(i, I);
    sf[P & 1][i] = __builtin_amdgcn_raw_buffer_load_b128(rs, lane * 16 + w3 * 1024, ((4 * P + 2 * rh + ((i & 3) >> 1)) * 16 + 8 * (i >> 2) + 4 * (i & 1)) * 1024, 16);
  };
  auto sload = [&](auto PP) AI { sfor<0, 8>([&](auto I) AI { sload1(PP, I); }); };
  auto flagld = [&](int P) { return __hip_atomic_load(flags + P * 32 + (lane & 7) * FST, RLX_AGENT); };
  auto flagck = [&](int P, u64 v, u64 tag) AI {
    if (!__all(v == tag)) {
      unsigned spins = 0;
#pragma unroll 1
      do { __builtin_amdgcn_s_sleep(PSL); v = flagld(P); } while (!__all(v == tag) && ++spins < (1u << 20));
    }
    asm volatile("" ::: "memory");
  };
  auto signal = [&](int w) {
    waitlgkm<0>();
    if (lane == 0) lds_inc(misc + w * 4);
  };
  auto await = [&](int w, unsigned target = 4) {
    volatile LAS unsigned* words = (volatile LAS unsigned*)(smem + MISC_OFF);
#pragma unroll 1
    for (unsigned spins = 0; spins < (1u << 24); spins++) {
      if ((unsigned)__builtin_amdgcn_readfirstlane(words[w]) == target) break;
      __builtin_amdgcn_s_sleep(1);
    }
    asm volatile("" ::: "memory");
  };

  if (wave < 4) {
    const int ng = wave;
    __builtin_amdgcn_s_setprio(2);
    if (ng == 1) {
      const auto rc = __builtin_amdgcn_make_buffer_rsrc((void*)pc0, 0, CODE_LINES * 128, 0x00020000);
      __builtin_amdgcn_raw_ptr_buffer_load_lds(rc, smem + PFS_OFF, 4, ((bid >> 3) * 8 + (lane & 7)) * 128, 0, 0, 0);
    }
    const auto rout = __builtin_amdgcn_make_buffer_rsrc((void*)(out + (size_t)(batch * T_ + t0) * 1024), 0, TILE * 1024, 0x00020000);
    const u64 tag = __hip_atomic_load(epoch, RLX_AGENT) + 0x9E3779B97F4A7C15ull;
    {
      const auto rdw = __builtin_amdgcn_make_buffer_rsrc((void*)dw, 0, 31 * 1024, 0x00020000);
      unsigned tap[11];
      const int vo = (10 * ng - 4) * 1024 + (64 * j + lane) * 2;
#pragma unroll
      for (int m = 0; m < 11; m++) {
        int o = vo + m * 1024;
        asm volatile("" : "+v"(o));
        tap[m] = __builtin_amdgcn_raw_buffer_load_b16(rdw, o, 0, 0);
      }
      LAS unsigned* tb = (LAS unsigned*)(smem + TAB_OFF + lane * TS);
#pragma unroll
      for (int m = 0; m < 10; m++) tb[5 * ng + (m & 1) * 20 + (m >> 1)] = tap[m] | (tap[m + 1] << 16);
      tb[40 + ng] = 0u;
      if (threadIdx.x < 16) *(LAS unsigned*)(smem + MISC_OFF + threadIdx.x * 4) = 0u;
      if (threadIdx.x == 0) *(LAS u64*)(smem + MISC_OFF + 64) = tag;
    }
    const Base2 abase(smem + (l15 >> 1) * 1024 + (l15 >> 2) * 64 + (l15 & 1) * 512 + ((q ^ (l15 & 3)) * 16));
    constexpr int RH = 8 * 1024 + 4 * 64;
    const int q4 = l15 >> 2, p4 = lane & 3;
    const Base3 wa(smem + (8 * q + q4) * 256 + ((ng ^ q4 ^ ((q & 1) << 2)) * 32) + p4 * 8);
    const Base3 wg(smem + (8 * q + q4) * 256 + (((4 + ng) ^ q4 ^ ((q & 1) << 2)) * 32) + p4 * 8);
    LAS char* hbase = opaque(smem + HT_OFF + (ng * 16 + l15) * HS + q * 16);
    LAS char* hwrap = opaque(hbase - (q > 0 ? 256 : 0));
    bf16x8 Ba[16], Bg[16];
    bf16x8 A[PF][2];
    f32x4 ca[3][2], cgt[3][2];
    float e[8];
    u32x4 hp;

    auto loadW = [&](auto R) {
      CI(r, R);
      constexpr int ks = r >> 1, off = slot_of(wunit(ks >> 1)) + (ks & 1) * 8192;
      if constexpr (r & 1) Bg[ks] = cat8(ldtr(wg.at<off>()), ldtr(wg.at<off + 1024>()));
      else Ba[ks] = cat8(ldtr(wa.at<off>()), ldtr(wa.at<off + 1024>()));
    };
    auto glu = [&](auto U, auto PP, auto BP) {
      CI(u0, U); CI(pp, PP); CI(bp, BP);
      constexpr int u = u0 == 0 ? 0 : u0 == 1 ? -1 : u0 - 1;
      if constexpr (u >= 0 && u < 4) { e[2 * u] = vnl2e(cgt[pp][u >> 1][(2 * u) & 3]); e[2 * u + 1] = vnl2e(cgt[pp][u >> 1][(2 * u + 1) & 3]); }
      if constexpr (u >= 4 && u < 12) e[u - 4] = vexp2(e[u - 4]);
      if constexpr (u >= 12 && u < 16) { e[2 * u - 24] = vadd1(e[2 * u - 24]); e[2 * u - 23] = vadd1(e[2 * u - 23]); }
      if constexpr (u >= 16 && u < 24) e[u - 16] = vrcp(e[u - 16]);
      if constexpr (u >= 24 && u < 28) {
        constexpr int i = 2 * u - 48;
        e[i] = vmul(e[i], ca[pp][i >> 2][i & 3]); e[i + 1] = vmul(e[i + 1], ca[pp][i >> 2][(i & 3) + 1]);
      }
      if constexpr (u == 28 || u == 29) { constexpr int i = 2 * u - 56; hp[i] = vpk(e[2 * i], e[2 * i + 1]); hp[i + 1] = vpk(e[2 * i + 2], e[2 * i + 3]); }
      constexpr int t = (bp * 32) & 127;
      if constexpr (u == 30) *(LAS u32x4*)(hbase + t * 2) = hp;
      if constexpr (u == 32) {
        if constexpr (t == 64) {
          *(LAS u32x2*)(hwrap + 256 + 248) = (u32x2){hp[0], hp[1]};
          *(LAS u32x2*)(hbase + 256) = (u32x2){hp[2], hp[3]};
        } else {
          *(LAS u32x2*)(hbase + 256 + ((t + 60) & 127) * 2) = (u32x2){hp[0], hp[1]};
          *(LAS u32x2*)(hbase + 256 + ((t + 60) & 127) * 2 + 8) = (u32x2){hp[2], hp[3]};
        }
      }
    };
    auto step = [&](auto I, auto PP, auto KS, auto FIRST, auto NOFF, auto&& f0, auto&& f1) {
      CI(i, I); CI(pp, PP); CI(ks, KS); CI(first, FIRST); CI(noff, NOFF);
      if constexpr (first) mf0(ca[pp][0], A[i][0], Ba[ks]); else mf(ca[pp][0], A[i][0], Ba[ks]);
      sfence(); f0(); sfence();
      if constexpr (first) mf0(cgt[pp][0], A[i][0], Bg[ks]); else mf(cgt[pp][0], A[i][0], Bg[ks]);
      sfence();
      if constexpr (noff >= 0) A[i][0] = ldsA(abase.at<noff>());
      sfence();
      if constexpr (first) mf0(cgt[pp][1], A[i][1], Bg[ks]); else mf(cgt[pp][1], A[i][1], Bg[ks]);
      sfence(); f1(); sfence();
      if constexpr (first) mf0(ca[pp][1], A[i][1], Ba[ks]); else mf(ca[pp][1], A[i][1], Ba[ks]);
      sfence();
      if constexpr (noff >= 0) A[i][1] = ldsA(abase.at<noff + RH>());
      sfence();
    };
    auto nop = [] {};
    auto primeA = [&](auto N) {
      CI(n, N);
      sfor<0, PF>([&](auto I) { CI(i, I); A[i][0] = ldsA(abase.at<slot_of(n) + i * 64>()); A[i][1] = ldsA(abase.at<slot_of(n) + i * 64 + RH>()); });
    };

    waitlgkm<0>();
    bar();
    bar();
    ST(1);
    sfor<0, 4>(loadW);
    primeA(IC<4>{});
    sfor<0, 16>([&](auto S) {
      CI(s, S);
      constexpr int n = s + PF;
      step(IC<s % PF>{}, IC<(s >> 3)>{}, IC<(s & 7)>{}, IC<(s & 7) == 0>{}, IC<(n < 16 ? slot_of(4 + (n >> 3)) + (n & 7) * 64 : -1)>{},
           [&] { if constexpr (s < 6) loadW(IC<2 * s + 4>{}); },
           [&] { if constexpr (s < 6) loadW(IC<2 * s + 5>{}); });
    });
    ST(2);
    bar();
    bar();
    ST(3);
    sfor<16, 20>(loadW);
    primeA(IC<10>{});
    sfor<0, 16>([&](auto S) {
      CI(s, S);
      constexpr int n = s + PF;
      if constexpr (s == 8 - PF || s == 16 - PF) bar();
      step(IC<s % PF>{}, IC<(s >> 3)>{}, IC<8 + (s & 7)>{}, IC<0>{}, IC<slot_of(10 + (n >> 3)) + (n & 7) * 64>{},
           [&] { if constexpr (s < 6) loadW(IC<2 * s + 20>{});
                 if constexpr (s == 8) nops<7>();
                 if constexpr (s >= 8) { glu(IC<4 * (s - 8)>{}, IC<0>{}, IC<0>{}); glu(IC<4 * (s - 8) + 1>{}, IC<0>{}, IC<0>{}); } },
           [&] { if constexpr (s < 6) loadW(IC<2 * s + 21>{});
                 if constexpr (s >= 8) { glu(IC<4 * (s - 8) + 2>{}, IC<0>{}, IC<0>{}); glu(IC<4 * (s - 8) + 3>{}, IC<0>{}, IC<0>{}); } });
    });
    ST(5);
    sfor<2, 7>([&](auto BB) {
      CI(b, BB);
      sfor<0, 16>([&](auto S) {
        CI(s, S);
        constexpr int n = s + PF, gs = 16 * (b - 1) + s;
        if constexpr (s == 3) { waitlgkm<(b & 3) == 0 ? 4 : 4>(); bar(); }
        CST
        auto fill = [&](auto G) {
          CI(gp, G);
          if constexpr (b > 2 || gp > 0) glu(IC<gp + 31>{}, IC<(b & 1)>{}, IC<b - 2>{});
          glu(IC<gp - 1>{}, IC<(b & 1) ^ 1>{}, IC<b - 1>{});
        };
        step(IC<gs % PF>{}, IC<(b & 1)>{}, IC<s>{}, IC<s == 0>{}, IC<slot_of(8 + 2 * b + (n >> 3)) + (n & 7) * 64>{},
             [&] { fill(IC<2 * s>{}); }, [&] { fill(IC<2 * s + 1>{}); });
      });
    });
    const Base2 vb(smem + (8 * q + q4) * 256 + ((ng ^ q4) * 64) + p4 * 16);
    auto loadV = [&](auto R) {
      CI(r, R);
      constexpr int ks = r >> 1, off = slot_of(26 + (ks >> 1)) + (ks & 1) * 8192 + (r & 1) * 8;
      if constexpr (r & 1) Bg[ks] = cat8(ldtr(vb.at<off>()), ldtr(vb.at<off + 1024>()));
      else Ba[ks] = cat8(ldtr(vb.at<off>()), ldtr(vb.at<off + 1024>()));
    };
    sfor<0, 32>([&](auto S) {
      CI(s, S);
      constexpr int n = s + PF, gs = 96 + s, set = 1 + ((s >> 3) & 1), ks = (s >> 4) * 8 + (s & 7);
      if constexpr (s == 3 || s == 19) { waitlgkm<4>(); bar(); }
      if constexpr (s == 27) { waitlgkm<5>(); if (lane == 0) lds_inc(misc + 10 * 4); }
      auto fill = [&](auto G) {
        CI(gp, G);
        glu(IC<gp + 31>{}, IC<1>{}, IC<5>{});
        glu(IC<gp - 1>{}, IC<0>{}, IC<6>{});
        if constexpr (gp >= 38 && gp < 50) loadV(IC<gp - 38>{});
        if constexpr (gp >= 50) glu(IC<gp - 50>{}, IC<1>{}, IC<7>{});
      };
      step(IC<gs % PF>{}, IC<set>{}, IC<ks>{}, IC<ks == 0>{}, IC<(n < 32 ? slot_of(22 + (n >> 3)) + (n & 7) * 64 : -1)>{},
           [&] { fill(IC<2 * s>{}); }, [&] { fill(IC<2 * s + 1>{}); });
    });
    ST(6);
    u64 fv = flagld(0);
    sfor<14, 32>([&](auto U) { glu(U, IC<1>{}, IC<7>{}); });
    volatile LAS unsigned* words = (volatile LAS unsigned*)(smem + MISC_OFF);
    unsigned fl = words[8];
    sfor<0, 31>([&](auto U) { CI(u, U); glu(IC<u + 32>{}, IC<1>{}, IC<7>{}); glu(U, IC<2>{}, IC<8>{}); });
    if (__builtin_amdgcn_readfirstlane(fl) != 4) await(8);
    asm volatile("" ::: "memory");
    sfor<31, 34>([&](auto U) { glu(U, IC<2>{}, IC<8>{}); });
    waitlgkm<0>();
    bar();
    ST(7);
    const Base2 sbase(smem + lane * 16);
    const int tt = 4 * ((l15 >> 1) & 3) + 2 * (l15 >> 3) + (l15 & 1);
    const int ovo = (rh * 32 + tt) * 1024 + cg * 256 + ng * 64 + q * 16;
    unsigned op[4];
    auto ostep = [&](auto U, auto PP, auto PC) {
      CI(u, U); CI(pp, PP); CI(pc, PC);
      constexpr int hf = u / 3, s = u % 3;
      if constexpr (u < 6) {
        if constexpr (s == 0) { op[0] = vpk(ca[pp][hf][0], ca[pp][hf][1]); op[1] = vpk(ca[pp][hf][2], ca[pp][hf][3]); }
        if constexpr (s == 1) { op[2] = vpk(cgt[pp][hf][0], cgt[pp][hf][1]); op[3] = vpk(cgt[pp][hf][2], cgt[pp][hf][3]); }
        if constexpr (s == 2) __builtin_amdgcn_raw_buffer_store_b128((u32x4){op[0], op[1], op[2], op[3]}, rout, ovo + (pc * 64 + hf * 16) * 1024, 0, 0);
      }
    };
    __builtin_amdgcn_s_setprio(0);
    const Base2 swb(smem + ng * 1024 + lane * 16);
    auto swrite1 = [&](auto PP, auto I) { CI(P, PP); CI(i, I); *(LAS u32x4*)(swb.at<sbuf(P, i >> 2) + (i & 3) * 4096>()) = sf[P & 1][i]; };
    flagck(0, fv, tag);
    sload(IC<0>{});
    fv = flagld(1);
    sfor<12, 20>(loadV);
    sfence();
    sfor<0, 4>([&](auto I) { CI(i, I); issueV(33, i); });
    flagck(1, fv, tag);
    ST(14);
    sfor<0, 8>([&](auto I) { swrite1(IC<0>{}, I); sload1(IC<1>{}, I); sfence(); });
    waitvm<8>();
    signal(4);
    await(4, 8);
    ST(8);
    sfor<0, PF>([&](auto I) { CI(i, I); A[i][0] = ldsA(sbase.at<sfrag(0, 0, i)>()); A[i][1] = ldsA(sbase.at<sfrag(0, 1, i)>()); });
    sfor<0, 64>([&](auto S) {
      CI(gs, S);
      constexpr int P = gs >> 4, s = gs & 15, i = gs % PF, pp = P & 1, n = gs + PF;
      constexpr int idx = (n == 16 || n == 32 || n == 48) ? (n >> 4) : -1;
      constexpr int n2 = n + 1, idx2 = (n2 == 16 || n2 == 32 || n2 == 48) ? (n2 >> 4) : -1;
      if constexpr (idx >= 0) {
        if (__builtin_amdgcn_readfirstlane(fl) != 4) await(4 + idx);
        asm volatile("" ::: "memory");
        if constexpr (idx > 0) ST(8 + idx);
      }
      if constexpr (s == 0) mf0(ca[pp][0], Ba[s], A[i][0]); else mf(ca[pp][0], Ba[s], A[i][0]);
      sfence();
      if constexpr (P > 0 && s >= 2) ostep(IC<s - 2>{}, IC<pp ^ 1>{}, IC<P - 1>{});
      if constexpr (gs < 6) loadV(IC<20 + 2 * gs>{});
      if constexpr (gs >= 6 && gs < 10) swrite1(IC<1>{}, IC<2 * gs - 12>{});
      if constexpr (idx2 >= 0) fl = words[4 + idx2];
      sfence();
      if constexpr (s == 0) mf0(cgt[pp][0], Bg[s], A[i][0]); else mf(cgt[pp][0], Bg[s], A[i][0]);
      sfence();
      if constexpr (n < 64) A[i][0] = ldsA(sbase.at<sfrag((n & 63) >> 4, 0, n & 15)>());
      sfence();
      if constexpr (s == 0) mf0(cgt[pp][1], Bg[s], A[i][1]); else mf(cgt[pp][1], Bg[s], A[i][1]);
      sfence();
      if constexpr (gs < 6) loadV(IC<21 + 2 * gs>{});
      if constexpr (gs >= 6 && gs < 10) swrite1(IC<1>{}, IC<2 * gs - 11>{});
      if constexpr (gs == 11) { waitlgkm<4>(); if (lane == 0) lds_inc(misc + 5 * 4); }
      if constexpr (gs == 17) { if (lane == 0) lds_inc(misc + 9 * 4); }
      sfence();
      if constexpr (s == 0) mf0(ca[pp][1], Ba[s], A[i][1]); else mf(ca[pp][1], Ba[s], A[i][1]);
      sfence();
      if constexpr (n < 64) A[i][1] = ldsA(sbase.at<sfrag((n & 63) >> 4, 1, n & 15)>());
      sfence();
    });
    ST(12);
    nops<7>();
    sfor<0, 6>([&](auto U) { ostep(U, IC<1>{}, IC<3>{}); nops<1>(); });
    ST(13);
    STDUMP();
  } else {
    const int hw = wave - 4;
    const auto rx = __builtin_amdgcn_make_buffer_rsrc((void*)(x + (size_t)batch * (T_ * 1024)), 0, T_ * 1024, 0x00020000);
    const auto rw1 = __builtin_amdgcn_make_buffer_rsrc((void*)w1, 0, 1024 * 1024, 0x00020000);
    const int lc = (l15 >> 1) ^ q ^ ((hw >> 1) << 2);
    const int vw = (4 * hw + q) * 2048 + (lc >> 2) * 1024 + j * 128 + (lc & 3) * 32 + (lane & 1) * 16;
    const int vx = (t0 - 15 + 8 * (hw >> 1) + 2 * (hw & 1) + (lane >> 5)) * 1024 + (((((lane & 31) ^ (2 * (hw & 1) + (lane >> 5))) * 16) + KROT * 128) & 511);
    const int vx1 = vx + 32768;
    auto issue1 = [&](int n, int i) {
      if (n < 4 || (n >= 6 && n < 10)) {
        const int m = n < 4 ? n : n - 2;
        __builtin_amdgcn_raw_ptr_buffer_load_lds(rw1, smem + slot_of(n) + hw * 1024 + i * 4096, 16, vw, (((m & 4) + ((m + KROT) & 3)) * 64 + 16 * i) * 2048, 0, 0);
      } else if (n < 26) {
        const int b = n == 4 || n == 10 ? 0 : n == 5 || n == 11 ? 1 : n < 22 ? (n - 8) >> 1 : 7 + (n & 1);
        const int half = n < 6 ? 0 : n < 12 ? 1 : n < 22 ? (n & 1) : (n - 22) >> 1;
        LAS char* dst = smem + slot_of(n) + hw * 1024 + (hw >> 1) * 64 + i * (4096 + 128);
        if (b == 0) __builtin_amdgcn_raw_ptr_buffer_load_lds(rx, dst, 16, vx + half * 512 + (16 * (i & 1) + 4 * (i >> 1)) * 1024, 0, 0, 0);
        else __builtin_amdgcn_raw_ptr_buffer_load_lds(rx, dst, 16, vx1, (b - 1) * 32768 + half * 512 + (16 * (i & 1) + 4 * (i >> 1)) * 1024, 0, 0);
      } else {
        issueV(n, i);
      }
    };
    auto issue = [&](int n) {
#pragma unroll
      for (int i = 0; i < 4; i++) issue1(n, i);
    };
    auto warmx = [&](int r0, int rpw, int l0, int nl) {
      const int r = lane / nl;
      const int o = r < rpw ? (t0 - 15 + r0 + (4 * j + hw) * rpw + r) * 1024 + (l0 + lane % nl) * 128 : -1;
      __builtin_amdgcn_raw_ptr_buffer_load_lds(rx, smem + PFS_OFF, 4, o, 0, 0, 0);
    };
    auto warmv = [&] {
      __builtin_amdgcn_raw_ptr_buffer_load_lds(rw2, smem + PFS_OFF, 4, lane < 32 ? (((bid >> 3) * 4 + hw) * 32 + lane) * 128 : -1, 0, 0, 0);
    };
    STA;
    issue(0);
    WA
    issue(1); issue(2); issue(3); issue(4); issue(5); issue(6);
    WB
    bar();
    const u64 tag = *(LAS u64*)(smem + MISC_OFF + 64);
    bf16x8 T1[16], T2[4];
    {
      const int e4 = l15 >> 2, b3 = lane & 3;
      LAS char* tb = smem + TAB_OFF + 16 * hw * TS;
      const int i1 = (b3 & 1) * 20 + ((8 * q - b3 + 4) >> 1), i2 = (b3 & 1) * 20 + ((36 - b3) >> 1);
      sfor<0, 4>([&](auto EE) {
        CI(ee, EE);
        LAS unsigned* p = (LAS unsigned*)opaque(tb + 4 * ee * TS + (e4 == ee ? i1 : 40) * 4);
        sfor<0, 4>([&](auto QD) {
          CI(qd, QD);
          const u32x4 v = {p[qd * 44], p[qd * 44 + 1], p[qd * 44 + 2], p[qd * 44 + 3]};
          sfence();
          T1[4 * ee + qd] = __builtin_bit_cast(bf16x8, v);
        });
      });
      LAS unsigned* p = (LAS unsigned*)opaque(tb + 4 * q * TS + (e4 == q ? i2 : 40) * 4);
      sfor<0, 4>([&](auto QD) { CI(qd, QD); const u32x4 v = {0u, 0u, p[qd * 44], p[qd * 44 + 1]}; sfence(); T2[qd] = __builtin_bit_cast(bf16x8, v); });
    }
    LAS char* c1[2]; LAS char* c2[2];
    sfor<0, 2>([&](auto PP) {
      CI(pp, PP);
      const int sh = (l15 & 1) * 60 + 64 * pp + 4 * l15;
      c1[pp] = opaque(smem + HT_OFF + hw * HREG + (l15 & 1) * 256 + ((sh + 8 * q) & 127) * 2);
      const int op = (l15 & 1) ^ 1;
      c2[pp] = opaque(smem + HT_OFF + hw * HREG + 4 * q * HS + op * 256 + ((op * 60 + 64 * pp + 4 * l15 + 28) & 127) * 2);
    });
    waitvm<4 + WBN>();
    STA;
    bar();
    STA;
    issue(7); issue(8);
    STA;
    bar();
    STA;
    issue(9); issue(10); issue(11); issue(12);
    waitvm<8>();
    STA;
    bar();
    STA;
    issue(13);
    waitvm<8>();
    STA;
    bar();
    STA;
    issue(14);
    WC
    waitvm<8 + WCN>();
    STA;
    bar();
    STA;
    issue(15); issue(16);
    waitvm<8 + WCN>();
    STA;
    bar();
    STA;
    issue(17); issue(18);
    WD
    waitvm<8 + WDN>();
    STA;
    bar();
    STA;
    issue(19); issue(20);
    WE
    f32x4 d[4];
    unsigned sp[4][2];
    bf16x8 W[20];
    float sv[8], stt[8];
    auto convrd1 = [&](auto PP, auto K) {
      CI(P, PP); CI(k, K);
      constexpr int hh = k / 10, ee = (k % 10) >> 1, qq = k & 1, qd = 2 * hh + qq;
      W[10 * hh + 5 * qq + ee] = ldsA(ee < 4 ? c1[P & 1] + (4 * ee + qd) * HS : c2[P & 1] + qd * HS);
      sfence();
    };
    auto lo = [] { sfence(); __builtin_amdgcn_s_setprio(0); sfence(); };
    auto hi = [] { sfence(); __builtin_amdgcn_s_setprio(3); sfence(); };
    auto convmm = [&](auto HH, auto&& f) {
      CI(hh, HH);
      hi();
      sfor<0, 10>([&](auto K) {
        CI(k, K);
        constexpr int ee = k >> 1, qq = k & 1, qd = 2 * hh + qq;
        const bf16x8& t = ee < 4 ? T1[(4 * ee + qd) & 15] : T2[qd];
        d[qd] = __builtin_amdgcn_mfma_f32_16x16x32_bf16(t, W[10 * hh + 5 * qq + ee], ee == 0 ? (f32x4){0.f, 0.f, 0.f, 0.f} : d[qd], 0, 0, 0);
        sfence(); f(K); sfence();
      });
      sfence();
    };
    auto none = [](auto) {};
    auto shadowEA = [&](auto K) { CI(k, K); if constexpr (k < 8) stt[k] = vexp2(stt[k]); else { stt[2 * k - 16] = vadd1(stt[2 * k - 16]); stt[2 * k - 15] = vadd1(stt[2 * k - 15]); } };
    auto phM = [&](auto HH) { CI(hh, HH); sfor<0, 8>([&](auto I) { CI(i, I); sv[i] = d[2 * hh + (i & 1)][i >> 1]; stt[i] = sv[i] * -1.4426950408889634f; sfence(); }); };
    auto head = [&](auto PP, auto&& g) AI {
      sfor<0, 10>([&](auto K) { convrd1(PP, K); g(K); sfence(); });
      convmm(IC<0>{}, [&](auto K) { CI(k, K); convrd1(PP, IC<10 + k>{}); });
      phM(IC<0>{}); convmm(IC<1>{}, shadowEA); lo();
    };
    auto phE = [&](auto&& g) { sfor<0, 8>([&](auto I) { CI(i, I); stt[i] = vexp2(stt[i]); g(I); sfence(); }); };
    auto phA = [&](auto B0) { CI(b0, B0); sfor<b0, 8>([&](auto I) { CI(i, I); stt[i] = vadd1(stt[i]); }); sfence(); };
    auto phR = [&](auto&& g) { sfor<0, 8>([&](auto I) { CI(i, I); stt[i] = vrcp(stt[i]); g(I); sfence(); }); };
    auto phF = [&](auto HH) {
      CI(hh, HH);
      sfor<0, 8>([&](auto I) { CI(i, I); stt[i] = vmul(stt[i], sv[i]); });
      sfor<0, 4>([&](auto R) { CI(r, R); sp[r][hh] = pk2(stt[2 * r], stt[2 * r + 1]); });
      sfence();
    };
    auto dma = [&](int n) { return [&issue1, n](auto I) { CI(i, I); if constexpr (i & 1) issue1(n, i >> 1); }; };
    auto sstore = [&](auto PP) {
      CI(P, PP);
      sfor<0, 2>([&](auto I) {
        CI(i, I);
        auto r0 = __builtin_amdgcn_permlane16_swap(sp[2 * i][0], sp[2 * i + 1][0], false, false);
        auto r1 = __builtin_amdgcn_permlane16_swap(sp[2 * i][1], sp[2 * i + 1][1], false, false);
        __builtin_amdgcn_raw_buffer_store_b128((u32x4){r0[0], r1[0], r0[1], r1[1]}, rs, svo + P * 65536 + i * 128, 0, 16);
      });
    };
    const Base2 swb(smem + hw * 1024 + lane * 16);
    auto swrite1 = [&](auto PP, auto I) { CI(P, PP); CI(i, I); *(LAS u32x4*)(swb.at<sbuf(P, i >> 2) + (i & 3) * 4096>()) = sf[P & 1][i]; };
    auto dma8 = [&](int n) { return [&issue1, n](auto I) { CI(i, I); issue1(n + (i >> 2), i & 3); }; };
    auto dma3 = [&](int n) { return [&issue1, n](auto K) { CI(k, K); if constexpr (k == 1 || k == 4 || k == 7) issue1(n, 1 + k / 3); }; };
    auto flagst = [&](int P) { if (threadIdx.x == 256) __hip_atomic_store(flags + P * 32 + j * FST, tag, RLX_AGENT); };
    auto publish = [&](int P) {
      if (lane == 0) {
        if (lds_inc_rtn(misc + P * 4) == 3) __hip_atomic_store(flags + P * 32 + j * FST, tag, RLX_AGENT);
      }
    };
    auto dmah = [&](int n) { return [&issue1, n](auto K) { CI(k, K); if constexpr (k < 8) issue1(n + (k >> 2), k & 3); }; };
    waitvm<8 + WDN + WEN>();
    STA;
    bar();
    STA;
    head(IC<0>{}, dmah(21));
    phA(IC<4>{}); phR(none); phF(IC<0>{}); hi(); phM(IC<1>{}); lo();
    waitvm<8>();
    STA;
    bar();
    STA;
    phE(dma8(23)); phA(IC<0>{}); phR(none); phF(IC<1>{}); sstore(IC<0>{});
    waitvm<10>();
    STA;
    bar();
    STA;
    head(IC<1>{}, dmah(25));
    waitvm<8>();
    publish(0);
    phA(IC<4>{}); phR(none); phF(IC<0>{}); hi(); phM(IC<1>{}); lo();
    STA;
    bar();
    STA;
    phE(dma8(27)); phA(IC<0>{}); phR(none); phF(IC<1>{});
    sstore(IC<1>{});
    waitvm<2>();
    STA;
    bar();
    STA;
    head(IC<2>{}, [&](auto K) { CI(k, K); if constexpr (k >= 6) issue1(29, k - 6); });
    signal(8);
    phA(IC<4>{}); phR(dma(30));
    waitvm<8>();
    publish(1);
    phF(IC<0>{}); phM(IC<1>{});
    await(10);
    STA;
    phE(dma(31)); phA(IC<0>{}); phR(dma(32)); phF(IC<1>{}); sstore(IC<2>{});
    waitvm<10>();
    hi();
    STA;
    bar();
    STA;
    head(IC<3>{}, none);
    hi();
    waitvm<2>();
    if (lane == 0) lds_inc(misc + 4 * 4);
    phA(IC<4>{}); phR(none); phF(IC<0>{}); phM(IC<1>{}); phE(none); phA(IC<0>{}); phR(none); phF(IC<1>{});
    waitvm<0>();
    publish(2);
    sstore(IC<3>{});
    STA;
    waitvm<0>();
    publish(3);
    STA;
    flagck(2, ~tag, tag);
    STA;
    sload(IC<2>{});
    flagck(3, ~tag, tag);
    STA;
    await(9);
    sfor<0, 8>([&](auto I) { swrite1(IC<2>{}, I); sload1(IC<3>{}, I); sfence(); });
    signal(6);
    STA;
    if (j == 0 && threadIdx.x == 256) __hip_atomic_store(epoch, tag, RLX_AGENT);
    sfor<0, 8>([&](auto I) { swrite1(IC<3>{}, I); });
    signal(7);
    STA;
    STDUMP();
  }
}

extern "C" void kernel_launch(void* const* d_in, const int* in_sizes, int n_in, void* d_out, int out_size, void* d_ws,
                              size_t ws_size, hipStream_t stream) {
  (void)hipFuncSetAttribute((const void*)conv_module_kernel, hipFuncAttributeMaxDynamicSharedMemorySize, LDS_BYTES);
  conv_module_kernel<<<256, 512, LDS_BYTES, stream>>>((const char*)d_in[0], (const char*)d_in[1], (const char*)d_in[2],
                                                      (const char*)d_in[3], (char*)d_out, (char*)d_ws);
}
```

```cpp
#include <hip/hip_runtime.h>
#include <hip/hip_bf16.h>

typedef short s16x4 __attribute__((ext_vector_type(4)));
typedef __bf16 bf16x8 __attribute__((ext_vector_type(8)));
typedef __bf16 bf16x2 __attribute__((ext_vector_type(2)));
typedef float f32x4 __attribute__((ext_vector_type(4)));
typedef float f32x2 __attribute__((ext_vector_type(2)));
typedef unsigned u32x4 __attribute__((ext_vector_type(4)));
typedef unsigned u32x2 __attribute__((ext_vector_type(2)));
typedef unsigned long long u64;
#define LAS __attribute__((address_space(3)))
#define GAS __attribute__((address_space(1)))
#define DEVI __device__ __forceinline__
#define AI __attribute__((always_inline))
#define RLX_AGENT __ATOMIC_RELAXED, __HIP_MEMORY_SCOPE_AGENT

constexpr int T_ = 4000, TILE = 250;
constexpr int SLOT = 16896;
constexpr int NS = 7;
constexpr int HT_OFF = NS * SLOT;
constexpr int HS = 528;
constexpr int HREG = 16 * HS;
constexpr int TAB_OFF = HT_OFF + 64 * HS;
constexpr int TS = 176;
constexpr int MISC_OFF = TAB_OFF + 64 * TS;
constexpr int LDS_BYTES = 160 * 1024;
static_assert(MISC_OFF + 512 <= LDS_BYTES && 64 * HS >= 2 * SLOT);
constexpr size_t WS_FLAG = 8u << 20;
constexpr size_t WS_EPOCH = WS_FLAG + 32 * 4 * 8 * 128;
constexpr size_t WS_STAMP = WS_EPOCH + 32 * 128;
constexpr unsigned CODE_LINES = 208;
constexpr int PF = 3;

#define KROT gi
#define WA warmx(0, 2, 0, 4);
#define WB warmx(0, 2, 4, 4); warmx(64, 2, 0, 8);
#define WBN 2
#define WC warmx(128, 2, 0, 8);
#define WCN 1
#define WD warmx(192, 3, 0, 8);
#define WDN 1
#define WE warmv();
#define WEN 1
#define PSL 2
#define FST 1
#define CST
template <int N> DEVI void waitvm() { asm volatile("s_waitcnt vmcnt(%0)" ::"n"(N) : "memory"); }
template <int N> DEVI void waitlgkm() { asm volatile("s_waitcnt lgkmcnt(%0)" ::"n"(N) : "memory"); }
DEVI void bar() { asm volatile("" ::: "memory"); __builtin_amdgcn_s_barrier(); asm volatile("" ::: "memory"); }
DEVI void sfence() { __builtin_amdgcn_sched_barrier(0); }
DEVI unsigned pk2(float lo, float hi) { f32x2 p = {lo, hi}; return __builtin_bit_cast(unsigned, __builtin_convertvector(p, bf16x2)); }
DEVI bf16x8 ldsA(LAS char* p) { return *(LAS bf16x8*)p; }
DEVI s16x4 ld64(LAS char* p) { return *(LAS s16x4*)p; }
DEVI s16x4 ldtr(LAS char* p) { return __builtin_amdgcn_ds_read_tr16_b64_v4i16((LAS s16x4*)p); }
DEVI bf16x8 cat8(s16x4 a, s16x4 b) { return __builtin_bit_cast(bf16x8, __builtin_shufflevector(a, b, 0, 1, 2, 3, 4, 5, 6, 7)); }
DEVI void mf(f32x4& c, const bf16x8& a, const bf16x8& b) { asm volatile("v_mfma_f32_16x16x32_bf16 %0, %1, %2, %0" : "+v"(c) : "v"(a), "v"(b)); }
DEVI void mf0(f32x4& c, const bf16x8& a, const bf16x8& b) { asm volatile("v_mfma_f32_16x16x32_bf16 %0, %1, %2, 0" : "=&v"(c) : "v"(a), "v"(b)); }
DEVI float vnl2e(float a) { float r; asm volatile("v_mul_f32 %0, 0xbfb8aa3b, %1" : "=v"(r) : "v"(a)); return r; }
DEVI float vadd1(float a) { float r; asm volatile("v_add_f32 %0, 1.0, %1" : "=v"(r) : "v"(a)); return r; }
DEVI float vmul(float a, float b) { float r; asm volatile("v_mul_f32 %0, %1, %2" : "=v"(r) : "v"(a), "v"(b)); return r; }
DEVI float vexp2(float a) { float r; asm volatile("v_exp_f32 %0, %1" : "=v"(r) : "v"(a)); return r; }
DEVI float vrcp(float a) { float r; asm volatile("v_rcp_f32 %0, %1" : "=v"(r) : "v"(a)); return r; }
DEVI unsigned vpk(float a, float b) { unsigned r; asm volatile("v_cvt_pk_bf16_f32 %0, %1, %2" : "=v"(r) : "v"(a), "v"(b)); return r; }
DEVI LAS char* opaque(LAS char* p) { asm volatile("" : "+v"(p)); return p; }
DEVI void lds_inc(unsigned addr) { asm volatile("ds_add_u32 %0, %1" ::"v"(addr), "v"(1u) : "memory"); }
DEVI unsigned lds_inc_rtn(unsigned addr) { unsigned r; asm volatile("ds_add_rtn_u32 %0, %1, %2\n\ts_waitcnt lgkmcnt(0)" : "=&v"(r) : "v"(addr), "v"(1u) : "memory"); return r; }
DEVI unsigned umin_(unsigned a, unsigned b) { return a < b ? a : b; }
DEVI unsigned umax_(unsigned a, unsigned b) { return a > b ? a : b; }
template <int N> DEVI void nops() { asm volatile("s_nop %0" ::"n"(N)); }

template <int V> struct IC { static constexpr int value = V; };
template <int B, int E, class F> DEVI void sfor(F&& f) {
  if constexpr (B < E) { f(IC<B>{}); sfor<B + 1, E>(f); }
}
#define CI(name, arg) constexpr int name = decltype(arg)::value
template <int NB> struct BaseN {
  LAS char* p[NB];
  DEVI BaseN(LAS char* b) {
    p[0] = b;
#pragma unroll
    for (int i = 1; i < NB; i++) p[i] = opaque(b + i * 61440);
  }
  template <int OFF> DEVI LAS char* at() const {
    constexpr int k = OFF / 61440 < NB ? OFF / 61440 : NB - 1;
    static_assert(OFF - k * 61440 < 65536);
    return p[k] + (OFF - k * 61440);
  }
};
typedef BaseN<2> Base2;
typedef BaseN<3> Base3;

#define ST(i) do {} while (0)
#define STA do {} while (0)
#define STDUMP() do {} while (0)

constexpr int wunit(int m) { return m < 4 ? m : m + 2; }
constexpr int slot_of(int n) {
  return n < 7 ? n * SLOT : n < 9 ? HT_OFF + (n - 7) * SLOT : n < 31 ? ((n - 2) % NS) * SLOT : n < 33 ? slot_of(n - 5) : slot_of(25);
}
constexpr int sbuf(int P, int h) { return slot_of(P == 1 ? 29 + h : P == 2 ? 26 + h : h ? 24 : 28); }
constexpr int sfrag(int P, int hf, int ks) { return sbuf(P, ks >> 3) + (hf * 8 + (ks & 7)) * 1024; }
constexpr int PFS_OFF = MISC_OFF + 256;

__global__ __launch_bounds__(512, 2) void conv_module_kernel(const char* __restrict__ x, const char* __restrict__ w1,
                                                             const char* __restrict__ dw, const char* __restrict__ w2,
                                                             char* __restrict__ out, char* __restrict__ ws) {
  extern __shared__ __attribute__((aligned(16))) char smem_[];
  LAS char* smem = (LAS char*)smem_;
  u64 pc0;
  asm volatile("s_getpc_b64 %0" : "=s"(pc0));
  const int bid = blockIdx.x;
  const int gi = bid >> 6, j = (bid >> 3) & 7, g = (bid & 7) * 4 + gi;
  const int wave = __builtin_amdgcn_readfirstlane(threadIdx.x >> 6);
  const int lane = threadIdx.x & 63;
  const int batch = g >> 4, t0 = (g & 15) * TILE;
  const int cg = j & 3, rh = j >> 2;
  const int q = lane >> 4, l15 = lane & 15;
  const unsigned misc = (unsigned)(size_t)(smem + MISC_OFF);
  GAS u64* epoch = (GAS u64*)(ws + WS_EPOCH + g * 128);
  ST(0);
  const auto rs = __builtin_amdgcn_make_buffer_rsrc((void*)(ws + (size_t)g * 262144), 0, 262144, 0x00020000);
  GAS u64* flags = (GAS u64*)(ws + WS_FLAG + (size_t)g * 4096);
  const int svo = ((l15 >> 2) * 16 + 2 * j + ((wave >> 1) & 1)) * 1024 + ((2 * (wave & 1) + (q >> 1)) * 16 + 2 * (l15 & 3) + (q & 1)) * 16;

  const int w3 = wave & 3;
  const auto rw2 = __builtin_amdgcn_make_buffer_rsrc((void*)w2, 0, 512 * 1024, 0x00020000);
  const int vv = (4 * w3 + q) * 1024 + cg * 256 + (((l15 >> 2) ^ q) * 64) + (lane & 3) * 16;
  auto issueV = [&](int n, int i) {
    __builtin_amdgcn_raw_ptr_buffer_load_lds(rw2, smem + slot_of(n) + w3 * 1024 + i * 4096, 16, vv, ((n - 26) * 64 + 16 * i) * 1024, 0, 0);
  };
  u32x4 sf[2][8];
  auto sload1 = [&](auto PP, auto I) AI {
    CI(P, PP); CI(i, I);
    sf[P & 1][i] = __builtin_amdgcn_raw_buffer_load_b128(rs, lane * 16 + w3 * 1024, ((4 * P + 2 * rh + ((i & 3) >> 1)) * 16 + 8 * (i >> 2) + 4 * (i & 1)) * 1024, 16);
  };
  auto sload = [&](auto PP) AI { sfor<0, 8>([&](auto I) AI { sload1(PP, I); }); };
  auto flagld = [&](int P) { return __hip_atomic_load(flags + P * 32 + (lane & 7) * FST, RLX_AGENT); };
  auto flagck = [&](int P, u64 v, u64 tag) AI {
    if (!__all(v == tag)) {
      unsigned spins = 0;
#pragma unroll 1
      do { __builtin_amdgcn_s_sleep(PSL); v = flagld(P); } while (!__all(v == tag) && ++spins < (1u << 20));
    }
    asm volatile("" ::: "memory");
  };
  auto signal = [&](int w) {
    waitlgkm<0>();
    if (lane == 0) lds_inc(misc + w * 4);
  };
  auto await = [&](int w, unsigned target = 4) {
    volatile LAS unsigned* words = (volatile LAS unsigned*)(smem + MISC_OFF);
#pragma unroll 1
    for (unsigned spins = 0; spins < (1u << 24); spins++) {
      if ((unsigned)__builtin_amdgcn_readfirstlane(words[w]) == target) break;
      __builtin_amdgcn_s_sleep(1);
    }
    asm volatile("" ::: "memory");
  };

  if (wave < 4) {
    const int ng = wave;
    __builtin_amdgcn_s_setprio(2);
    if (ng == 1) {
      const auto rc = __builtin_amdgcn_make_buffer_rsrc((void*)pc0, 0, CODE_LINES * 128, 0x00020000);
      __builtin_amdgcn_raw_ptr_buffer_load_lds(rc, smem + PFS_OFF, 4, ((bid >> 3) * 8 + (lane & 7)) * 128, 0, 0, 0);
    }
    const auto rout = __builtin_amdgcn_make_buffer_rsrc((void*)(out + (size_t)(batch * T_ + t0) * 1024), 0, TILE * 1024, 0x00020000);
    const u64 tag = __hip_atomic_load(epoch, RLX_AGENT) + 0x9E3779B97F4A7C15ull;
    {
      const auto rdw = __builtin_amdgcn_make_buffer_rsrc((void*)dw, 0, 31 * 1024, 0x00020000);
      unsigned tap[11];
      const int vo = (10 * ng - 4) * 1024 + (64 * j + lane) * 2;
#pragma unroll
      for (int m = 0; m < 11; m++) {
        int o = vo + m * 1024;
        asm volatile("" : "+v"(o));
        tap[m] = __builtin_amdgcn_raw_buffer_load_b16(rdw, o, 0, 0);
      }
      LAS unsigned* tb = (LAS unsigned*)(smem + TAB_OFF + lane * TS);
#pragma unroll
      for (int m = 0; m < 10; m++) tb[5 * ng + (m & 1) * 20 + (m >> 1)] = tap[m] | (tap[m + 1] << 16);
      tb[40 + ng] = 0u;
      if (threadIdx.x < 16) *(LAS unsigned*)(smem + MISC_OFF + threadIdx.x * 4) = 0u;
      if (threadIdx.x == 0) *(LAS u64*)(smem + MISC_OFF + 64) = tag;
    }
    const Base2 abase(smem + (l15 >> 1) * 1024 + (l15 >> 2) * 64 + (l15 & 1) * 512 + ((q ^ (l15 & 3)) * 16));
    constexpr int RH = 8 * 1024 + 4 * 64;
    const int q4 = l15 >> 2, p4 = lane & 3;
    const Base3 wa(smem + (8 * q + q4) * 256 + ((ng ^ q4 ^ ((q & 1) << 2)) * 32) + p4 * 8);
    const Base3 wg(smem + (8 * q + q4) * 256 + (((4 + ng) ^ q4 ^ ((q & 1) << 2)) * 32) + p4 * 8);
    LAS char* hbase = opaque(smem + HT_OFF + (ng * 16 + l15) * HS + q * 16);
    LAS char* hwrap = opaque(hbase - (q > 0 ? 256 : 0));
    bf16x8 Ba[16], Bg[16];
    bf16x8 A[PF][2];
    f32x4 ca[3][2], cgt[3][2];
    float e[8];
    u32x4 hp;

    auto loadW = [&](auto R) {
      CI(r, R);
      constexpr int ks = r >> 1, off = slot_of(wunit(ks >> 1)) + (ks & 1) * 8192;
      if constexpr (r & 1) Bg[ks] = cat8(ldtr(wg.at<off>()), ldtr(wg.at<off + 1024>()));
      else Ba[ks] = cat8(ldtr(wa.at<off>()), ldtr(wa.at<off + 1024>()));
    };
    auto glu = [&](auto U, auto PP, auto BP) {
      CI(u0, U); CI(pp, PP); CI(bp, BP);
      constexpr int u = u0 == 0 ? 0 : u0 == 1 ? -1 : u0 - 1;
      if constexpr (u >= 0 && u < 4) { e[2 * u] = vnl2e(cgt[pp][u >> 1][(2 * u) & 3]); e[2 * u + 1] = vnl2e(cgt[pp][u >> 1][(2 * u + 1) & 3]); }
      if constexpr (u >= 4 && u < 12) e[u - 4] = vexp2(e[u - 4]);
      if constexpr (u >= 12 && u < 16) { e[2 * u - 24] = vadd1(e[2 * u - 24]); e[2 * u - 23] = vadd1(e[2 * u - 23]); }
      if constexpr (u >= 16 && u < 24) e[u - 16] = vrcp(e[u - 16]);
      if constexpr (u >= 24 && u < 28) {
        constexpr int i = 2 * u - 48;
        e[i] = vmul(e[i], ca[pp][i >> 2][i & 3]); e[i + 1] = vmul(e[i + 1], ca[pp][i >> 2][(i & 3) + 1]);
      }
      if constexpr (u == 28 || u == 29) { constexpr int i = 2 * u - 56; hp[i] = vpk(e[2 * i], e[2 * i + 1]); hp[i + 1] = vpk(e[2 * i + 2], e[2 * i + 3]); }
      constexpr int t = (bp * 32) & 127;
      if constexpr (u == 30) *(LAS u32x4*)(hbase + t * 2) = hp;
      if constexpr (u == 32) {
        if constexpr (t == 64) {
          *(LAS u32x2*)(hwrap + 256 + 248) = (u32x2){hp[0], hp[1]};
          *(LAS u32x2*)(hbase + 256) = (u32x2){hp[2], hp[3]};
        } else {
          *(LAS u32x2*)(hbase + 256 + ((t + 60) & 127) * 2) = (u32x2){hp[0], hp[1]};
          *(LAS u32x2*)(hbase + 256 + ((t + 60) & 127) * 2 + 8) = (u32x2){hp[2], hp[3]};
        }
      }
    };
    auto step = [&](auto I, auto PP, auto KS, auto FIRST, auto NOFF, auto&& f0, auto&& f1) {
      CI(i, I); CI(pp, PP); CI(ks, KS); CI(first, FIRST); CI(noff, NOFF);
      if constexpr (first) mf0(ca[pp][0], A[i][0], Ba[ks]); else mf(ca[pp][0], A[i][0], Ba[ks]);
      sfence(); f0(); sfence();
      if constexpr (first) mf0(cgt[pp][0], A[i][0], Bg[ks]); else mf(cgt[pp][0], A[i][0], Bg[ks]);
      sfence();
      if constexpr (noff >= 0) A[i][0] = ldsA(abase.at<noff>());
      sfence();
      if constexpr (first) mf0(cgt[pp][1], A[i][1], Bg[ks]); else mf(cgt[pp][1], A[i][1], Bg[ks]);
      sfence(); f1(); sfence();
      if constexpr (first) mf0(ca[pp][1], A[i][1], Ba[ks]); else mf(ca[pp][1], A[i][1], Ba[ks]);
      sfence();
      if constexpr (noff >= 0) A[i][1] = ldsA(abase.at<noff + RH>());
      sfence();
    };
    auto nop = [] {};
    auto primeA = [&](auto N) {
      CI(n, N);
      sfor<0, PF>([&](auto I) { CI(i, I); A[i][0] = ldsA(abase.at<slot_of(n) + i * 64>()); A[i][1] = ldsA(abase.at<slot_of(n) + i * 64 + RH>()); });
    };

    waitlgkm<0>();
    bar();
    bar();
    ST(1);
    sfor<0, 4>(loadW);
    primeA(IC<4>{});
    sfor<0, 16>([&](auto S) {
      CI(s, S);
      constexpr int n = s + PF;
      step(IC<s % PF>{}, IC<(s >> 3)>{}, IC<(s & 7)>{}, IC<(s & 7) == 0>{}, IC<(n < 16 ? slot_of(4 + (n >> 3)) + (n & 7) * 64 : -1)>{},
           [&] { if constexpr (s < 6) loadW(IC<2 * s + 4>{}); },
           [&] { if constexpr (s < 6) loadW(IC<2 * s + 5>{}); });
    });
    ST(2);
    bar();
    bar();
    ST(3);
    sfor<16, 20>(loadW);
    primeA(IC<10>{});
    sfor<0, 16>([&](auto S) {
      CI(s, S);
      constexpr int n = s + PF;
      if constexpr (s == 8 - PF || s == 16 - PF) bar();
      step(IC<s % PF>{}, IC<(s >> 3)>{}, IC<8 + (s & 7)>{}, IC<0>{}, IC<slot_of(10 + (n >> 3)) + (n & 7) * 64>{},
           [&] { if constexpr (s < 6) loadW(IC<2 * s + 20>{});
                 if constexpr (s == 8) nops<7>();
                 if constexpr (s >= 8) { glu(IC<4 * (s - 8)>{}, IC<0>{}, IC<0>{}); glu(IC<4 * (s - 8) + 1>{}, IC<0>{}, IC<0>{}); } },
           [&] { if constexpr (s < 6) loadW(IC<2 * s + 21>{});
                 if constexpr (s >= 8) { glu(IC<4 * (s - 8) + 2>{}, IC<0>{}, IC<0>{}); glu(IC<4 * (s - 8) + 3>{}, IC<0>{}, IC<0>{}); } });
    });
    ST(5);
    sfor<2, 7>([&](auto BB) {
      CI(b, BB);
      sfor<0, 16>([&](auto S) {
        CI(s, S);
        constexpr int n = s + PF, gs = 16 * (b - 1) + s;
        if constexpr (s == 3) { waitlgkm<(b & 3) == 0 ? 4 : 4>(); bar(); }
        CST
        auto fill = [&](auto G) {
          CI(gp, G);
          if constexpr (b > 2 || gp > 0) glu(IC<gp + 31>{}, IC<(b & 1)>{}, IC<b - 2>{});
          glu(IC<gp - 1>{}, IC<(b & 1) ^ 1>{}, IC<b - 1>{});
        };
        step(IC<gs % PF>{}, IC<(b & 1)>{}, IC<s>{}, IC<s == 0>{}, IC<slot_of(8 + 2 * b + (n >> 3)) + (n & 7) * 64>{},
             [&] { fill(IC<2 * s>{}); }, [&] { fill(IC<2 * s + 1>{}); });
      });
    });
    const Base2 vb(smem + (8 * q + q4) * 256 + ((ng ^ q4) * 64) + p4 * 16);
    auto loadV = [&](auto R) {
      CI(r, R);
      constexpr int ks = r >> 1, off = slot_of(26 + (ks >> 1)) + (ks & 1) * 8192 + (r & 1) * 8;
      if constexpr (r & 1) Bg[ks] = cat8(ldtr(vb.at<off>()), ldtr(vb.at<off + 1024>()));
      else Ba[ks] = cat8(ldtr(vb.at<off>()), ldtr(vb.at<off + 1024>()));
    };
    sfor<0, 32>([&](auto S) {
      CI(s, S);
      constexpr int n = s + PF, gs = 96 + s, set = 1 + ((s >> 3) & 1), ks = (s >> 4) * 8 + (s & 7);
      if constexpr (s == 3 || s == 19) { waitlgkm<4>(); bar(); }
      if constexpr (s == 27) { waitlgkm<5>(); if (lane == 0) lds_inc(misc + 10 * 4); }
      auto fill = [&](auto G) {
        CI(gp, G);
        glu(IC<gp + 31>{}, IC<1>{}, IC<5>{});
        glu(IC<gp - 1>{}, IC<0>{}, IC<6>{});
        if constexpr (gp >= 38 && gp < 50) loadV(IC<gp - 38>{});
        if constexpr (gp >= 50) glu(IC<gp - 50>{}, IC<1>{}, IC<7>{});
      };
      step(IC<gs % PF>{}, IC<set>{}, IC<ks>{}, IC<ks == 0>{}, IC<(n < 32 ? slot_of(22 + (n >> 3)) + (n & 7) * 64 : -1)>{},
           [&] { fill(IC<2 * s>{}); }, [&] { fill(IC<2 * s + 1>{}); });
    });
    ST(6);
    u64 fv = flagld(0);
    sfor<14, 32>([&](auto U) { glu(U, IC<1>{}, IC<7>{}); });
    volatile LAS unsigned* words = (volatile LAS unsigned*)(smem + MISC_OFF);
    unsigned fl = words[8];
    sfor<0, 31>([&](auto U) { CI(u, U); glu(IC<u + 32>{}, IC<1>{}, IC<7>{}); glu(U, IC<2>{}, IC<8>{}); });
    if (__builtin_amdgcn_readfirstlane(fl) != 4) await(8);
    asm volatile("" ::: "memory");
    sfor<31, 34>([&](auto U) { glu(U, IC<2>{}, IC<8>{}); });
    waitlgkm<0>();
    bar();
    ST(7);
    const Base2 sbase(smem + lane * 16);
    const int tt = 4 * ((l15 >> 1) & 3) + 2 * (l15 >> 3) + (l15 & 1);
    const int ovo = (rh * 32 + tt) * 1024 + cg * 256 + ng * 64 + q * 16;
    unsigned op[4];
    auto ostep = [&](auto U, auto PP, auto PC) {
      CI(u, U); CI(pp, PP); CI(pc, PC);
      constexpr int hf = u / 3, s = u % 3;
      if constexpr (u < 6) {
        if constexpr (s == 0) { op[0] = vpk(ca[pp][hf][0], ca[pp][hf][1]); op[1] = vpk(ca[pp][hf][2], ca[pp][hf][3]); }
        if constexpr (s == 1) { op[2] = vpk(cgt[pp][hf][0], cgt[pp][hf][1]); op[3] = vpk(cgt[pp][hf][2], cgt[pp][hf][3]); }
        if constexpr (s == 2) __builtin_amdgcn_raw_buffer_store_b128((u32x4){op[0], op[1], op[2], op[3]}, rout, ovo + (pc * 64 + hf * 16) * 1024, 0, 16);
      }
    };
    __builtin_amdgcn_s_setprio(0);
    const Base2 swb(smem + ng * 1024 + lane * 16);
    auto swrite1 = [&](auto PP, auto I) { CI(P, PP); CI(i, I); *(LAS u32x4*)(swb.at<sbuf(P, i >> 2) + (i & 3) * 4096>()) = sf[P & 1][i]; };
    flagck(0, fv, tag);
    sload(IC<0>{});
    fv = flagld(1);
    sfor<12, 20>(loadV);
    sfence();
    sfor<0, 4>([&](auto I) { CI(i, I); issueV(33, i); });
    flagck(1, fv, tag);
    ST(14);
    sfor<0, 8>([&](auto I) { swrite1(IC<0>{}, I); sload1(IC<1>{}, I); sfence(); });
    waitvm<8>();
    signal(4);
    await(4, 8);
    ST(8);
    sfor<0, PF>([&](auto I) { CI(i, I); A[i][0] = ldsA(sbase.at<sfrag(0, 0, i)>()); A[i][1] = ldsA(sbase.at<sfrag(0, 1, i)>()); });
    sfor<0, 64>([&](auto S) {
      CI(gs, S);
      constexpr int P = gs >> 4, s = gs & 15, i = gs % PF, pp = P & 1, n = gs + PF;
      constexpr int idx = (n == 16 || n == 32 || n == 48) ? (n >> 4) : -1;
      constexpr int n2 = n + 1, idx2 = (n2 == 16 || n2 == 32 || n2 == 48) ? (n2 >> 4) : -1;
      if constexpr (idx >= 0) {
        if (__builtin_amdgcn_readfirstlane(fl) != 4) await(4 + idx);
        asm volatile("" ::: "memory");
        if constexpr (idx > 0) ST(8 + idx);
      }
      if constexpr (s == 0) mf0(ca[pp][0], Ba[s], A[i][0]); else mf(ca[pp][0], Ba[s], A[i][0]);
      sfence();
      if constexpr (P > 0 && s >= 2) ostep(IC<s - 2>{}, IC<pp ^ 1>{}, IC<P - 1>{});
      if constexpr (gs < 6) loadV(IC<20 + 2 * gs>{});
      if constexpr (gs >= 6 && gs < 10) swrite1(IC<1>{}, IC<2 * gs - 12>{});
      if constexpr (idx2 >= 0) fl = words[4 + idx2];
      sfence();
      if constexpr (s == 0) mf0(cgt[pp][0], Bg[s], A[i][0]); else mf(cgt[pp][0], Bg[s], A[i][0]);
      sfence();
      if constexpr (n < 64) A[i][0] = ldsA(sbase.at<sfrag((n & 63) >> 4, 0, n & 15)>());
      sfence();
      if constexpr (s == 0) mf0(cgt[pp][1], Bg[s], A[i][1]); else mf(cgt[pp][1], Bg[s], A[i][1]);
      sfence();
      if constexpr (gs < 6) loadV(IC<21 + 2 * gs>{});
      if constexpr (gs >= 6 && gs < 10) swrite1(IC<1>{}, IC<2 * gs - 11>{});
      if constexpr (gs == 11) { waitlgkm<4>(); if (lane == 0) lds_inc(misc + 5 * 4); }
      if constexpr (gs == 17) { if (lane == 0) lds_inc(misc + 9 * 4); }
      sfence();
      if constexpr (s == 0) mf0(ca[pp][1], Ba[s], A[i][1]); else mf(ca[pp][1], Ba[s], A[i][1]);
      sfence();
      if constexpr (n < 64) A[i][1] = ldsA(sbase.at<sfrag((n & 63) >> 4, 1, n & 15)>());
      sfence();
    });
    ST(12);
    nops<7>();
    sfor<0, 6>([&](auto U) { ostep(U, IC<1>{}, IC<3>{}); nops<1>(); });
    ST(13);
    STDUMP();
  } else {
    const int hw = wave - 4;
    const auto rx = __builtin_amdgcn_make_buffer_rsrc((void*)(x + (size_t)batch * (T_ * 1024)), 0, T_ * 1024, 0x00020000);
    const auto rw1 = __builtin_amdgcn_make_buffer_rsrc((void*)w1, 0, 1024 * 1024, 0x00020000);
    const int lc = (l15 >> 1) ^ q ^ ((hw >> 1) << 2);
    const int vw = (4 * hw + q) * 2048 + (lc >> 2) * 1024 + j * 128 + (lc & 3) * 32 + (lane & 1) * 16;
    const int vx = (t0 - 15 + 8 * (hw >> 1) + 2 * (hw & 1) + (lane >> 5)) * 1024 + (((((lane & 31) ^ (2 * (hw & 1) + (lane >> 5))) * 16) + KROT * 128) & 511);
    const int vx1 = vx + 32768;
    auto issue1 = [&](int n, int i) {
      if (n < 4 || (n >= 6 && n < 10)) {
        const int m = n < 4 ? n : n - 2;
        __builtin_amdgcn_raw_ptr_buffer_load_lds(rw1, smem + slot_of(n) + hw * 1024 + i * 4096, 16, vw, (((m & 4) + ((m + KROT) & 3)) * 64 + 16 * i) * 2048, 0, 0);
      } else if (n < 26) {
        const int b = n == 4 || n == 10 ? 0 : n == 5 || n == 11 ? 1 : n < 22 ? (n - 8) >> 1 : 7 + (n & 1);
        const int half = n < 6 ? 0 : n < 12 ? 1 : n < 22 ? (n & 1) : (n - 22) >> 1;
        LAS char* dst = smem + slot_of(n) + hw * 1024 + (hw >> 1) * 64 + i * (4096 + 128);
        if (b == 0) __builtin_amdgcn_raw_ptr_buffer_load_lds(rx, dst, 16, vx + half * 512 + (16 * (i & 1) + 4 * (i >> 1)) * 1024, 0, 0, 0);
        else __builtin_amdgcn_raw_ptr_buffer_load_lds(rx, dst, 16, vx1, (b - 1) * 32768 + half * 512 + (16 * (i & 1) + 4 * (i >> 1)) * 1024, 0, 0);
      } else {
        issueV(n, i);
      }
    };
    auto issue = [&](int n) {
#pragma unroll
      for (int i = 0; i < 4; i++) issue1(n, i);
    };
    auto warmx = [&](int r0, int rpw, int l0, int nl) {
      const int r = lane / nl;
      const int o = r < rpw ? (t0 - 15 + r0 + (4 * j + hw) * rpw + r) * 1024 + (l0 + lane % nl) * 128 : -1;
      __builtin_amdgcn_raw_ptr_buffer_load_lds(rx, smem + PFS_OFF, 4, o, 0, 0, 0);
    };
    auto warmv = [&] {
      __builtin_amdgcn_raw_ptr_buffer_load_lds(rw2, smem + PFS_OFF, 4, lane < 32 ? (((bid >> 3) * 4 + hw) * 32 + lane) * 128 : -1, 0, 0, 0);
    };
    STA;
    issue(0);
    WA
    issue(1); issue(2); issue(3); issue(4); issue(5); issue(6);
    WB
    bar();
    const u64 tag = *(LAS u64*)(smem + MISC_OFF + 64);
    bf16x8 T1[16], T2[4];
    {
      const int e4 = l15 >> 2, b3 = lane & 3;
      LAS char* tb = smem + TAB_OFF + 16 * hw * TS;
      const int i1 = (b3 & 1) * 20 + ((8 * q - b3 + 4) >> 1), i2 = (b3 & 1) * 20 + ((36 - b3) >> 1);
      sfor<0, 4>([&](auto EE) {
        CI(ee, EE);
        LAS unsigned* p = (LAS unsigned*)opaque(tb + 4 * ee * TS + (e4 == ee ? i1 : 40) * 4);
        sfor<0, 4>([&](auto QD) {
          CI(qd, QD);
          const u32x4 v = {p[qd * 44], p[qd * 44 + 1], p[qd * 44 + 2], p[qd * 44 + 3]};
          sfence();
          T1[4 * ee + qd] = __builtin_bit_cast(bf16x8, v);
        });
      });
      LAS unsigned* p = (LAS unsigned*)opaque(tb + 4 * q * TS + (e4 == q ? i2 : 40) * 4);
      sfor<0, 4>([&](auto QD) { CI(qd, QD); const u32x4 v = {0u, 0u, p[qd * 44], p[qd * 44 + 1]}; sfence(); T2[qd] = __builtin_bit_cast(bf16x8, v); });
    }
    LAS char* c1[2]; LAS char* c2[2];
    sfor<0, 2>([&](auto PP) {
      CI(pp, PP);
      const int sh = (l15 & 1) * 60 + 64 * pp + 4 * l15;
      c1[pp] = opaque(smem + HT_OFF + hw * HREG + (l15 & 1) * 256 + ((sh + 8 * q) & 127) * 2);
      const int op = (l15 & 1) ^ 1;
      c2[pp] = opaque(smem + HT_OFF + hw * HREG + 4 * q * HS + op * 256 + ((op * 60 + 64 * pp + 4 * l15 + 28) & 127) * 2);
    });
    waitvm<4 + WBN>();
    STA;
    bar();
    STA;
    issue(7); issue(8);
    STA;
    bar();
    STA;
    issue(9); issue(10); issue(11); issue(12);
    waitvm<8>();
    STA;
    bar();
    STA;
    issue(13);
    waitvm<8>();
    STA;
    bar();
    STA;
    issue(14);
    WC
    waitvm<8 + WCN>();
    STA;
    bar();
    STA;
    issue(15); issue(16);
    waitvm<8 + WCN>();
    STA;
    bar();
    STA;
    issue(17); issue(18);
    WD
    waitvm<8 + WDN>();
    STA;
    bar();
    STA;
    issue(19); issue(20);
    WE
    f32x4 d[4];
    unsigned sp[4][2];
    bf16x8 W[20];
    float sv[8], stt[8];
    auto convrd1 = [&](auto PP, auto K) {
      CI(P, PP); CI(k, K);
      constexpr int hh = k / 10, ee = (k % 10) >> 1, qq = k & 1, qd = 2 * hh + qq;
      W[10 * hh + 5 * qq + ee] = ldsA(ee < 4 ? c1[P & 1] + (4 * ee + qd) * HS : c2[P & 1] + qd * HS);
      sfence();
    };
    auto lo = [] { sfence(); __builtin_amdgcn_s_setprio(0); sfence(); };
    auto hi = [] { sfence(); __builtin_amdgcn_s_setprio(3); sfence(); };
    auto convmm = [&](auto HH, auto&& f) {
      CI(hh, HH);
      hi();
      sfor<0, 10>([&](auto K) {
        CI(k, K);
        constexpr int ee = k >> 1, qq = k & 1, qd = 2 * hh + qq;
        const bf16x8& t = ee < 4 ? T1[(4 * ee + qd) & 15] : T2[qd];
        d[qd] = __builtin_amdgcn_mfma_f32_16x16x32_bf16(t, W[10 * hh + 5 * qq + ee], ee == 0 ? (f32x4){0.f, 0.f, 0.f, 0.f} : d[qd], 0, 0, 0);
        sfence(); f(K); sfence();
      });
      sfence();
    };
    auto none = [](auto) {};
    auto shadowEA = [&](auto K) { CI(k, K); if constexpr (k < 8) stt[k] = vexp2(stt[k]); else { stt[2 * k - 16] = vadd1(stt[2 * k - 16]); stt[2 * k - 15] = vadd1(stt[2 * k - 15]); } };
    auto phM = [&](auto HH) { CI(hh, HH); sfor<0, 8>([&](auto I) { CI(i, I); sv[i] = d[2 * hh + (i & 1)][i >> 1]; stt[i] = sv[i] * -1.4426950408889634f; sfence(); }); };
    auto head = [&](auto PP, auto&& g) AI {
      sfor<0, 10>([&](auto K) { convrd1(PP, K); g(K); sfence(); });
      convmm(IC<0>{}, [&](auto K) { CI(k, K); convrd1(PP, IC<10 + k>{}); });
      phM(IC<0>{}); convmm(IC<1>{}, shadowEA); lo();
    };
    auto phE = [&](auto&& g) { sfor<0, 8>([&](auto I) { CI(i, I); stt[i] = vexp2(stt[i]); g(I); sfence(); }); };
    auto phA = [&](auto B0) { CI(b0, B0); sfor<b0, 8>([&](auto I) { CI(i, I); stt[i] = vadd1(stt[i]); }); sfence(); };
    auto phR = [&](auto&& g) { sfor<0, 8>([&](auto I) { CI(i, I); stt[i] = vrcp(stt[i]); g(I); sfence(); }); };
    auto phF = [&](auto HH) {
      CI(hh, HH);
      sfor<0, 8>([&](auto I) { CI(i, I); stt[i] = vmul(stt[i], sv[i]); });
      sfor<0, 4>([&](auto R) { CI(r, R); sp[r][hh] = pk2(stt[2 * r], stt[2 * r + 1]); });
      sfence();
    };
    auto dma = [&](int n) { return [&issue1, n](auto I) { CI(i, I); if constexpr (i & 1) issue1(n, i >> 1); }; };
    auto sstore = [&](auto PP) {
      CI(P, PP);
      sfor<0, 2>([&](auto I) {
        CI(i, I);
        auto r0 = __builtin_amdgcn_permlane16_swap(sp[2 * i][0], sp[2 * i + 1][0], false, false);
        auto r1 = __builtin_amdgcn_permlane16_swap(sp[2 * i][1], sp[2 * i + 1][1], false, false);
        __builtin_amdgcn_raw_buffer_store_b128((u32x4){r0[0], r1[0], r0[1], r1[1]}, rs, svo + P * 65536 + i * 128, 0, 16);
      });
    };
    const Base2 swb(smem + hw * 1024 + lane * 16);
    auto swrite1 = [&](auto PP, auto I) { CI(P, PP); CI(i, I); *(LAS u32x4*)(swb.at<sbuf(P, i >> 2) + (i & 3) * 4096>()) = sf[P & 1][i]; };
    auto dma8 = [&](int n) { return [&issue1, n](auto I) { CI(i, I); issue1(n + (i >> 2), i & 3); }; };
    auto dma3 = [&](int n) { return [&issue1, n](auto K) { CI(k, K); if constexpr (k == 1 || k == 4 || k == 7) issue1(n, 1 + k / 3); }; };
    auto flagst = [&](int P) { if (threadIdx.x == 256) __hip_atomic_store(flags + P * 32 + j * FST, tag, RLX_AGENT); };
    auto publish = [&](int P) {
      if (lane == 0) {
        if (lds_inc_rtn(misc + P * 4) == 3) __hip_atomic_store(flags + P * 32 + j * FST, tag, RLX_AGENT);
      }
    };
    auto dmah = [&](int n) { return [&issue1, n](auto K) { CI(k, K); if constexpr (k < 8) issue1(n + (k >> 2), k & 3); }; };
    waitvm<8 + WDN + WEN>();
    STA;
    bar();
    STA;
    head(IC<0>{}, dmah(21));
    phA(IC<4>{}); phR(none); phF(IC<0>{}); hi(); phM(IC<1>{}); lo();
    waitvm<8>();
    STA;
    bar();
    STA;
    phE(dma8(23)); phA(IC<0>{}); phR(none); phF(IC<1>{}); sstore(IC<0>{});
    waitvm<10>();
    STA;
    bar();
    STA;
    head(IC<1>{}, dmah(25));
    waitvm<8>();
    publish(0);
    phA(IC<4>{}); phR(none); phF(IC<0>{}); hi(); phM(IC<1>{}); lo();
    STA;
    bar();
    STA;
    phE(dma8(27)); phA(IC<0>{}); phR(none); phF(IC<1>{});
    sstore(IC<1>{});
    waitvm<2>();
    STA;
    bar();
    STA;
    head(IC<2>{}, [&](auto K) { CI(k, K); if constexpr (k >= 6) issue1(29, k - 6); });
    signal(8);
    phA(IC<4>{}); phR(dma(30));
    waitvm<8>();
    publish(1);
    phF(IC<0>{}); phM(IC<1>{});
    await(10);
    STA;
    phE(dma(31)); phA(IC<0>{}); phR(dma(32)); phF(IC<1>{}); sstore(IC<2>{});
    waitvm<10>();
    hi();
    STA;
    bar();
    STA;
    head(IC<3>{}, none);
    hi();
    waitvm<2>();
    if (lane == 0) lds_inc(misc + 4 * 4);
    phA(IC<4>{}); phR(none); phF(IC<0>{}); phM(IC<1>{}); phE(none); phA(IC<0>{}); phR(none); phF(IC<1>{});
    waitvm<0>();
    publish(2);
    sstore(IC<3>{});
    STA;
    waitvm<0>();
    publish(3);
    STA;
    flagck(2, ~tag, tag);
    STA;
    sload(IC<2>{});
    flagck(3, ~tag, tag);
    STA;
    await(9);
    sfor<0, 8>([&](auto I) { swrite1(IC<2>{}, I); sload1(IC<3>{}, I); sfence(); });
    signal(6);
    STA;
    if (j == 0 && threadIdx.x == 256) __hip_atomic_store(epoch, tag, RLX_AGENT);
    sfor<0, 8>([&](auto I) { swrite1(IC<3>{}, I); });
    signal(7);
    STA;
    STDUMP();
  }
}

extern "C" void kernel_launch(void* const* d_in, const int* in_sizes, int n_in, void* d_out, int out_size, void* d_ws,
                              size_t ws_size, hipStream_t stream) {
  (void)hipFuncSetAttribute((const void*)conv_module_kernel, hipFuncAttributeMaxDynamicSharedMemorySize, LDS_BYTES);
  conv_module_kernel<<<256, 512, LDS_BYTES, stream>>>((const char*)d_in[0], (const char*)d_in[1], (const char*)d_in[2],
                                                      (const char*)d_in[3], (char*)d_out, (char*)d_ws);
}
```

```cpp
#include <hip/hip_runtime.h>
#include <hip/hip_bf16.h>

typedef short s16x4 __attribute__((ext_vector_type(4)));
typedef __bf16 bf16x8 __attribute__((ext_vector_type(8)));
typedef __bf16 bf16x2 __attribute__((ext_vector_type(2)));
typedef float f32x4 __attribute__((ext_vector_type(4)));
typedef float f32x2 __attribute__((ext_vector_type(2)));
typedef unsigned u32x4 __attribute__((ext_vector_type(4)));
typedef unsigned u32x2 __attribute__((ext_vector_type(2)));
typedef unsigned long long u64;
#define LAS __attribute__((address_space(3)))
#define GAS __attribute__((address_space(1)))
#define DEVI __device__ __forceinline__
#define AI __attribute__((always_inline))
#define RLX_AGENT __ATOMIC_RELAXED, __HIP_MEMORY_SCOPE_AGENT

constexpr int T_ = 4000, TILE = 250;
constexpr int SLOT = 16896;
constexpr int NS = 7;
constexpr int HT_OFF = NS * SLOT;
constexpr int HS = 592;
constexpr int HREG = 16 * HS;
constexpr int TAB_OFF = HT_OFF + 2 * SLOT;
constexpr int TS = 176;
constexpr int MISC_OFF = TAB_OFF + 64 * TS;
constexpr int LDS_BYTES = 160 * 1024;
static_assert(MISC_OFF + 512 <= LDS_BYTES && HT_OFF + 64 * HS <= MISC_OFF);
constexpr size_t WS_FLAG = 8u << 20;
constexpr size_t WS_EPOCH = WS_FLAG + 32 * 4 * 8 * 128;
constexpr size_t WS_STAMP = WS_EPOCH + 32 * 128;
constexpr unsigned CODE_LINES = 208;
constexpr unsigned EMPTY = 0xFFFFFFFFu;
constexpr int PF = 3;

#define KROT gi
#define PSL 2
#define FST 16
#define S0T 1
#define HLO lo();
#define BROT 1
#define SLP
#define ORD 0
#define MARKS mark(4); mark(5);
#define SLPN 8
#define TSPLIT 3
#define CST
template <int N> DEVI void waitvm() { asm volatile("s_waitcnt vmcnt(%0)" ::"n"(N) : "memory"); }
template <int N> DEVI void waitlgkm() { asm volatile("s_waitcnt lgkmcnt(%0)" ::"n"(N) : "memory"); }
DEVI void bar() { asm volatile("" ::: "memory"); __builtin_amdgcn_s_barrier(); asm volatile("" ::: "memory"); }
DEVI void sfence() { __builtin_amdgcn_sched_barrier(0); }
DEVI unsigned pk2(float lo, float hi) { f32x2 p = {lo, hi}; return __builtin_bit_cast(unsigned, __builtin_convertvector(p, bf16x2)); }
DEVI bf16x8 ldsA(LAS char* p) { return *(LAS bf16x8*)p; }
DEVI s16x4 ld64(LAS char* p) { return *(LAS s16x4*)p; }
DEVI s16x4 ldtr(LAS char* p) { return __builtin_amdgcn_ds_read_tr16_b64_v4i16((LAS s16x4*)p); }
DEVI bf16x8 cat8(s16x4 a, s16x4 b) { return __builtin_bit_cast(bf16x8, __builtin_shufflevector(a, b, 0, 1, 2, 3, 4, 5, 6, 7)); }
DEVI void mf(f32x4& c, const bf16x8& a, const bf16x8& b) { asm volatile("v_mfma_f32_16x16x32_bf16 %0, %1, %2, %0" : "+v"(c) : "v"(a), "v"(b)); }
DEVI void mf0(f32x4& c, const bf16x8& a, const bf16x8& b) { asm volatile("v_mfma_f32_16x16x32_bf16 %0, %1, %2, 0" : "=&v"(c) : "v"(a), "v"(b)); }
DEVI float vnl2e(float a) { float r; asm volatile("v_mul_f32 %0, 0xbfb8aa3b, %1" : "=v"(r) : "v"(a)); return r; }
DEVI float vadd1(float a) { float r; asm volatile("v_add_f32 %0, 1.0, %1" : "=v"(r) : "v"(a)); return r; }
DEVI float vmul(float a, float b) { float r; asm volatile("v_mul_f32 %0, %1, %2" : "=v"(r) : "v"(a), "v"(b)); return r; }
DEVI float vexp2(float a) { float r; asm volatile("v_exp_f32 %0, %1" : "=v"(r) : "v"(a)); return r; }
DEVI float vrcp(float a) { float r; asm volatile("v_rcp_f32 %0, %1" : "=v"(r) : "v"(a)); return r; }
DEVI unsigned vpk(float a, float b) { unsigned r; asm volatile("v_cvt_pk_bf16_f32 %0, %1, %2" : "=v"(r) : "v"(a), "v"(b)); return r; }
DEVI LAS char* opaque(LAS char* p) { asm volatile("" : "+v"(p)); return p; }
DEVI void lds_inc(unsigned addr) { asm volatile("ds_add_u32 %0, %1" ::"v"(addr), "v"(1u) : "memory"); }
DEVI unsigned lds_inc_rtn(unsigned addr) { unsigned r; asm volatile("ds_add_rtn_u32 %0, %1, %2\n\ts_waitcnt lgkmcnt(0)" : "=&v"(r) : "v"(addr), "v"(1u) : "memory"); return r; }
DEVI unsigned umin_(unsigned a, unsigned b) { return a < b ? a : b; }
DEVI unsigned umax_(unsigned a, unsigned b) { return a > b ? a : b; }
template <int N> DEVI void nops() { asm volatile("s_nop %0" ::"n"(N)); }

template <int V> struct IC { static constexpr int value = V; };
template <int B, int E, class F> DEVI void sfor(F&& f) {
  if constexpr (B < E) { f(IC<B>{}); sfor<B + 1, E>(f); }
}
#define CI(name, arg) constexpr int name = decltype(arg)::value
template <int NB> struct BaseN {
  LAS char* p[NB];
  DEVI BaseN(LAS char* b) {
    p[0] = b;
#pragma unroll
    for (int i = 1; i < NB; i++) p[i] = opaque(b + i * 61440);
  }
  template <int OFF> DEVI LAS char* at() const {
    constexpr int k = OFF / 61440 < NB ? OFF / 61440 : NB - 1;
    static_assert(OFF - k * 61440 < 65536);
    return p[k] + (OFF - k * 61440);
  }
};
typedef BaseN<2> Base2;
typedef BaseN<3> Base3;

#define ST(i) do {} while (0)
#define STA do {} while (0)
#define STR(k) do {} while (0)
#define STDUMP() do {} while (0)

constexpr int wunit(int m) { return m < 4 ? m : m + 2; }
constexpr int slot_of(int n) { return (n == 8 ? 1 : n == 10 ? 8 : n < 9 ? n : n < 31 ? (n - 2) % NS : (n - 7) % NS) * SLOT; }
constexpr int sbuf(int P, int h) { return slot_of(P == 0 ? 24 + h : P == 1 ? 29 + h : 26 + h); }
constexpr int sfrag(int P, int hf, int ks) {
  return P == 2 ? HT_OFF + (ks & 3) * HREG + (4 * (ks >> 3) + 2 * hf + ((ks >> 2) & 1)) * 1024 : sbuf(P, ks >> 3) + (hf * 8 + (ks & 7)) * 1024;
}
constexpr int PFS_OFF = MISC_OFF + 256;

__global__ __launch_bounds__(512, 2) void conv_module_kernel(const char* __restrict__ x, const char* __restrict__ w1,
                                                             const char* __restrict__ dw, const char* __restrict__ w2,
                                                             char* out, char* ws) {
  extern __shared__ __attribute__((aligned(16))) char smem_[];
  LAS char* smem = (LAS char*)smem_;
  u64 pc0;
  asm volatile("s_getpc_b64 %0" : "=s"(pc0));
  const int bid = blockIdx.x;
  const int gi = bid >> 6, j = (bid >> 3) & 7, g = (bid & 7) * 4 + gi;
  const int wave = __builtin_amdgcn_readfirstlane(threadIdx.x >> 6);
  const int lane = threadIdx.x & 63;
  const int batch = g >> 4, t0 = (g & 15) * TILE;
  const int cg = j & 3, rh = j >> 2;
  const int q = lane >> 4, l15 = lane & 15;
  const unsigned misc = (unsigned)(size_t)(smem + MISC_OFF);
  GAS u64* epoch = (GAS u64*)(ws + WS_EPOCH + g * 128);
  ST(0);
  const auto rs = __builtin_amdgcn_make_buffer_rsrc((void*)(ws + (size_t)g * 262144), 0, 262144, 0x00020000);
  GAS u64* flags = (GAS u64*)(ws + WS_FLAG + (size_t)g * 4096);
  const int svo = (q * 16 + 2 * j + ((wave >> 1) & 1)) * 1024 + ((2 * (wave & 1) + (l15 >> 3)) * 16 + (lane & 7)) * 16;

  const int w3 = wave & 3;
  if (wave == 1) {
    const auto rc = __builtin_amdgcn_make_buffer_rsrc((void*)pc0, 0, CODE_LINES * 128, 0x00020000);
    __builtin_amdgcn_raw_ptr_buffer_load_lds(rc, smem + PFS_OFF, 4, ((bid >> 3) * 8 + (lane & 7)) * 128, 0, 0, 0);
  }
  asm volatile("" ::: "memory");
  const auto rdw = __builtin_amdgcn_make_buffer_rsrc((void*)dw, 0, 31 * 1024, 0x00020000);
  if (wave < 4) __builtin_amdgcn_raw_ptr_buffer_load_lds(rdw, smem + TAB_OFF + w3 * 16 * TS, 16, (lane >> 1) * 1024 + j * 128 + w3 * 32 + (lane & 1) * 16, 0, 0, 0);
  u64 ep0 = 0;
  if (wave < 4) ep0 = __hip_atomic_load(epoch, RLX_AGENT);
  asm volatile("" ::: "memory");
  const int r5 = BROT ? (j >= 5 ? j - 5 : j) : 0, r4 = BROT ? j : 0, vrot = BROT ? 2 * gi + rh : 0;
  auto blk = [&](int b) { return b < 5 ? (b + r5 >= 5 ? b + r5 - 5 : b + r5) : 5 + ((b + r4) & 3); };
  const auto rw2 = __builtin_amdgcn_make_buffer_rsrc((void*)w2, 0, 512 * 1024, 0x00020000);
  const int vv = (4 * w3 + q) * 1024 + cg * 256 + (((l15 >> 2) ^ q) * 64) + (lane & 3) * 16;
  auto issueV = [&](int n, int i) {
    __builtin_amdgcn_raw_ptr_buffer_load_lds(rw2, smem + slot_of(n) + w3 * 1024 + i * 4096, 16, vv, (((n - 26 + vrot) & 7) * 64 + 16 * i) * 1024, 0, 0);
  };
  u32x4 sf[2][8];
  int slo = lane * 16;
  auto sload1 = [&](auto PP, auto I) AI {
    CI(P, PP); CI(i, I);
    sf[P & 1][i] = __builtin_amdgcn_raw_buffer_load_b128(rs, slo, ((4 * P + 2 * rh + ((i & 3) >> 1)) * 16 + ((8 * (i >> 2) + 4 * (i & 1) + w3 + 2 * vrot) & 15)) * 1024, 16);
  };
  auto sload = [&](auto PP) AI { sfor<0, 8>([&](auto I) AI { sload1(PP, I); }); };
  auto flagld = [&](int P) { return __hip_atomic_load(flags + P * 128 + (lane & 7) * FST, RLX_AGENT); };
  auto flagck = [&](int P, u64 v, u64 tag) AI {
    if (!__all(v == tag)) {
      unsigned spins = 0;
#pragma unroll 1
      do { __builtin_amdgcn_s_sleep(PSL); v = flagld(P); } while (!__all(v == tag) && ++spins < (1u << 20));
    }
    asm volatile("" ::: "memory");
  };
  auto sempty = [&](auto PP) AI {
    CI(P, PP);
    unsigned m = 0;
    sfor<0, 8>([&](auto I) AI { CI(i, I); m = umax_(umax_(m, sf[P & 1][i][0]), sf[P & 1][i][2]); });
    return __any(m == EMPTY);
  };
  auto scheck = [&](auto PP) AI {
    if (__builtin_expect(sempty(PP), 0)) {
      unsigned spins = 0;
#pragma unroll 1
      do {
        asm volatile("" : "+v"(slo) :: "memory");
        sload(PP);
        __builtin_amdgcn_s_waitcnt(0x0F70);
      } while (sempty(PP) && ++spins < (1u << 16));
    }
  };
  auto signal = [&](int w) {
    waitlgkm<0>();
    if (lane == 0) lds_inc(misc + w * 4);
  };
  auto await = [&](int w, unsigned target = 4) {
    volatile LAS unsigned* words = (volatile LAS unsigned*)(smem + MISC_OFF);
#pragma unroll 1
    for (unsigned spins = 0; spins < (1u << 24); spins++) {
      if ((unsigned)__builtin_amdgcn_readfirstlane(words[w]) == target) break;
      __builtin_amdgcn_s_sleep(1);
    }
    asm volatile("" ::: "memory");
  };

  if (wave < 4) {
    const int ng = wave;
    __builtin_amdgcn_s_setprio(2);
    const auto rout = __builtin_amdgcn_make_buffer_rsrc((void*)(out + (size_t)(batch * T_ + t0) * 1024), 0, TILE * 1024, 0x00020000);
    const u64 tag = ep0 + 0x9E3779B97F4A7C15ull;
    waitvm<0>();
    {
      LAS char* tb = smem + TAB_OFF + ng * 16 * TS;
      unsigned tap[13];
#pragma unroll
      for (int m = 0; m < 13; m++) {
        const unsigned o = 12 * q - 9 + m;
        tap[m] = *(LAS unsigned short*)(tb + (o < 31u ? o : 31u) * 32 + l15 * 2);
      }
      waitlgkm<0>();
      LAS unsigned* tw = (LAS unsigned*)(tb + 4 + l15 * TS) + 6 * q;
#pragma unroll
      for (int m = 0; m < 12; m++) tw[(m & 1) ? 19 + (m >> 1) : (m >> 1) - 1] = tap[m] | (tap[m + 1] << 16);
      asm volatile("" ::: "memory");
      if (threadIdx.x < 16) *(LAS unsigned*)(smem + MISC_OFF + threadIdx.x * 4) = 0u;
      if (threadIdx.x == 0) *(LAS u64*)(smem + MISC_OFF + 64) = tag;
    }
    const Base3 abase(smem + (l15 >> 1) * 1024 + (l15 >> 2) * 64 + (l15 & 1) * 512 + ((q ^ (l15 & 3)) * 16));
    constexpr int RH = 8 * 1024 + 4 * 64;
    const int q4 = l15 >> 2, p4 = lane & 3;
    const Base3 wa(smem + (8 * q + q4) * 256 + ((ng ^ q4 ^ ((q & 1) << 2)) * 32) + p4 * 8);
    const Base3 wg(smem + (8 * q + q4) * 256 + (((4 + ng) ^ q4 ^ ((q & 1) << 2)) * 32) + p4 * 8);
    LAS char* hbase = opaque(smem + HT_OFF + (ng * 16 + l15) * HS + q * 16);
    bf16x8 Ba[16], Bg[16];
    bf16x8 A[PF][2];
    f32x4 ca[3][2], cgt[3][2];
    float e[8];
    u32x4 hp, hp0;

    auto loadW = [&](auto R) {
      CI(r, R);
      constexpr int ks = r >> 1, off = slot_of(wunit(ks >> 1)) + (ks & 1) * 8192;
      if constexpr (r & 1) Bg[ks] = cat8(ldtr(wg.at<off>()), ldtr(wg.at<off + 1024>()));
      else Ba[ks] = cat8(ldtr(wa.at<off>()), ldtr(wa.at<off + 1024>()));
    };
    auto glu = [&](auto U, auto PP, auto BP) {
      CI(u0, U); CI(pp, PP); CI(bp, BP);
      constexpr int u = u0 == 0 ? 0 : u0 == 1 ? -1 : u0 - 1;
      if constexpr (u >= 0 && u < 4) { e[2 * u] = vnl2e(cgt[pp][u >> 1][(2 * u) & 3]); e[2 * u + 1] = vnl2e(cgt[pp][u >> 1][(2 * u + 1) & 3]); }
      if constexpr (u >= 4 && u < 12) e[u - 4] = vexp2(e[u - 4]);
      if constexpr (u >= 12 && u < 16) { e[2 * u - 24] = vadd1(e[2 * u - 24]); e[2 * u - 23] = vadd1(e[2 * u - 23]); }
      if constexpr (u >= 16 && u < 24) e[u - 16] = vrcp(e[u - 16]);
      if constexpr (u >= 24 && u < 28) {
        constexpr int i = 2 * u - 48;
        e[i] = vmul(e[i], ca[pp][i >> 2][i & 3]); e[i + 1] = vmul(e[i + 1], ca[pp][i >> 2][(i & 3) + 1]);
      }
      if constexpr (u == 28 || u == 29) { constexpr int i = 2 * u - 56; hp[i] = vpk(e[2 * i], e[2 * i + 1]); hp[i + 1] = vpk(e[2 * i + 2], e[2 * i + 3]); }
      if constexpr (u == 30) { if constexpr (bp == 0) hp0 = hp; else *(LAS u32x4*)(hbase + blk(bp) * 64) = hp; }
    };
    auto step = [&](auto I, auto PP, auto KS, auto FIRST, auto NOFF, auto&& f0, auto&& f1) {
      CI(i, I); CI(pp, PP); CI(ks, KS); CI(first, FIRST); CI(noff, NOFF);
      if constexpr (first) mf0(ca[pp][0], A[i][0], Ba[ks]); else mf(ca[pp][0], A[i][0], Ba[ks]);
      sfence(); f0(); sfence();
      if constexpr (first) mf0(cgt[pp][0], A[i][0], Bg[ks]); else mf(cgt[pp][0], A[i][0], Bg[ks]);
      sfence();
      if constexpr (noff >= 0) A[i][0] = ldsA(abase.at<noff>());
      sfence();
      if constexpr (first) mf0(cgt[pp][1], A[i][1], Bg[ks]); else mf(cgt[pp][1], A[i][1], Bg[ks]);
      sfence(); f1(); sfence();
      if constexpr (first) mf0(ca[pp][1], A[i][1], Ba[ks]); else mf(ca[pp][1], A[i][1], Ba[ks]);
      sfence();
      if constexpr (noff >= 0) A[i][1] = ldsA(abase.at<noff + RH>());
      sfence();
    };
    auto nop = [] {};
    auto primeA = [&](auto N) {
      CI(n, N);
      sfor<0, PF>([&](auto I) { CI(i, I); A[i][0] = ldsA(abase.at<slot_of(n) + i * 64>()); A[i][1] = ldsA(abase.at<slot_of(n) + i * 64 + RH>()); });
    };

    waitlgkm<0>();
    ST(4);
    bar();
    bar();
    ST(1);
    sfor<0, 4>(loadW);
    primeA(IC<4>{});
    sfor<0, 16>([&](auto S) {
      CI(s, S);
      constexpr int n = s + PF;
      step(IC<s % PF>{}, IC<(s >> 3)>{}, IC<(s & 7)>{}, IC<(s & 7) == 0>{}, IC<(n < 16 ? slot_of(4 + (n >> 3)) + (n & 7) * 64 : -1)>{},
           [&] { if constexpr (s < 6) loadW(IC<2 * s + 4>{}); },
           [&] { if constexpr (s < 6) loadW(IC<2 * s + 5>{}); });
    });
    ST(2);
    bar();
    bar();
    ST(3);
    sfor<16, 20>(loadW);
    primeA(IC<10>{});
    sfor<0, 16>([&](auto S) {
      CI(s, S);
      constexpr int n = s + PF;
      if constexpr (s == 8 - PF || s == 16 - PF) bar();
      step(IC<s % PF>{}, IC<(s >> 3)>{}, IC<8 + (s & 7)>{}, IC<0>{}, IC<slot_of(10 + (n >> 3)) + (n & 7) * 64>{},
           [&] { if constexpr (s < 6) loadW(IC<2 * s + 20>{});
                 if constexpr (s == 8) nops<7>();
                 if constexpr (s >= 8) { glu(IC<4 * (s - 8)>{}, IC<0>{}, IC<0>{}); glu(IC<4 * (s - 8) + 1>{}, IC<0>{}, IC<0>{}); } },
           [&] { if constexpr (s < 6) loadW(IC<2 * s + 21>{});
                 if constexpr (s >= 8) { glu(IC<4 * (s - 8) + 2>{}, IC<0>{}, IC<0>{}); glu(IC<4 * (s - 8) + 3>{}, IC<0>{}, IC<0>{}); } });
    });
    ST(5); STR(0);
    const Base2 vb(smem + (8 * q + q4) * 256 + ((ng ^ q4) * 64) + p4 * 16);
    auto loadV = [&](auto R) {
      CI(r, R);
      constexpr int ks = r >> 1, off = slot_of(26 + (ks >> 1)) + (ks & 1) * 8192 + (r & 1) * 8;
      if constexpr (r & 1) Bg[ks] = cat8(ldtr(vb.at<off>()), ldtr(vb.at<off + 1024>()));
      else Ba[ks] = cat8(ldtr(vb.at<off>()), ldtr(vb.at<off + 1024>()));
    };
    sfor<2, 7>([&](auto BB) {
      CI(b, BB);
      sfor<0, 16>([&](auto S) {
        CI(s, S);
        constexpr int n = s + PF, gs = 16 * (b - 1) + s;
        if constexpr (s == 3) { waitlgkm<6>(); bar(); }
        CST
        auto fill = [&](auto G) {
          CI(gp, G);
          if constexpr (b > 2 || gp > 0) glu(IC<gp + 31>{}, IC<(b & 1)>{}, IC<b - 2>{});
          glu(IC<gp - 1>{}, IC<(b & 1) ^ 1>{}, IC<b - 1>{});
          if constexpr (b == 3 && gp == 2) *(LAS u32x4*)(hbase + blk(0) * 64) = hp0;
        };
        step(IC<gs % PF>{}, IC<(b & 1)>{}, IC<s>{}, IC<s == 0>{}, IC<slot_of(8 + 2 * b + (n >> 3)) + (n & 7) * 64>{},
             [&] { fill(IC<2 * s>{}); }, [&] { fill(IC<2 * s + 1>{}); });
      });
    });
    u64 fv = 0;
    sfor<0, 32>([&](auto S) {
      CI(s, S);
      constexpr int n = s + PF, gs = 96 + s, set = 1 + ((s >> 3) & 1), ks = (s >> 4) * 8 + (s & 7);
      if constexpr (s == 3 || s == 19) bar();
      if constexpr (s == 27) { waitlgkm<5>(); if (lane == 0) lds_inc(misc + 10 * 4); }
      if constexpr (s == 31) fv = flagld(0);
      auto fill = [&](auto G) {
        CI(gp, G);
        glu(IC<gp + 31>{}, IC<1>{}, IC<5>{});
        glu(IC<gp - 1>{}, IC<0>{}, IC<6>{});
        if constexpr (gp >= 38 && gp < 50) loadV(IC<gp - 38>{});
        if constexpr (gp >= 50) glu(IC<gp - 50>{}, IC<1>{}, IC<7>{});
      };
      step(IC<gs % PF>{}, IC<set>{}, IC<ks>{}, IC<ks == 0>{}, IC<(n < 32 ? slot_of(22 + (n >> 3)) + (n & 7) * 64 : -1)>{},
           [&] { fill(IC<2 * s>{}); }, [&] { fill(IC<2 * s + 1>{}); });
    });
    ST(6);
    sfor<14, 32>([&](auto U) { glu(U, IC<1>{}, IC<7>{}); });
    volatile LAS unsigned* words = (volatile LAS unsigned*)(smem + MISC_OFF);
    unsigned fl = 0;
    sfor<0, 32>([&](auto U) { glu(U, IC<2>{}, IC<8>{}); });
    waitlgkm<0>();
    ST(17);
    bar();
    ST(7); STR(1);
    const Base3 sbase(smem + lane * 16);
    const int ovo = (rh * 64 + l15) * 1024 + cg * 256 + ng * 64 + q * 16;
    unsigned op[4];
    auto ostep = [&](auto U, auto PP, auto PC) {
      CI(u, U); CI(pp, PP); CI(pc, PC);
      constexpr int hf = u / 3, s = u % 3;
      if constexpr (u < 6) {
        if constexpr (s == 0) { op[0] = vpk(ca[pp][hf][0], ca[pp][hf][1]); op[1] = vpk(ca[pp][hf][2], ca[pp][hf][3]); }
        if constexpr (s == 1) { op[2] = vpk(cgt[pp][hf][0], cgt[pp][hf][1]); op[3] = vpk(cgt[pp][hf][2], cgt[pp][hf][3]); }
        if constexpr (s == 2) __builtin_amdgcn_raw_buffer_store_b128((u32x4){op[0], op[1], op[2], op[3]}, rout, ovo + ((pc >> 1) * 128 + hf * 32 + (pc & 1) * 16) * 1024, 0, 0);
      }
    };
    const Base2 swb(smem + ng * 1024 + lane * 16);
    auto swrite1 = [&](auto PP, auto I) { CI(P, PP); CI(i, I); *(LAS u32x4*)(swb.at<sbuf(P, i >> 2) + (i & 3) * 4096>()) = sf[P & 1][i]; };
    flagck(0, fv, tag);
    sload(IC<0>{});
    sfor<12, 20>(loadV);
    ST(14);
    sfor<0, 8>([&](auto I) { swrite1(IC<0>{}, I); });
    signal(4);
    sload(IC<1>{});
    ST(15);
    await(4, 8);
    ST(8);
    sfor<0, PF>([&](auto I) { CI(i, I); A[i][0] = ldsA(sbase.at<sfrag(0, 0, i)>()); A[i][1] = ldsA(sbase.at<sfrag(0, 1, i)>()); });
    sfor<0, 64>([&](auto S) {
      CI(gs, S);
      constexpr int P = gs >> 4, s = gs & 15, i = gs % PF, pp = P & 1, n = gs + PF;
      constexpr int idx = (n == 16 || n == 32 || n == 48) ? (n >> 4) : -1;
      constexpr int n2 = n + 1, idx2 = (n2 == 16 || n2 == 32 || n2 == 48) ? (n2 >> 4) : -1;
      if constexpr (idx >= 0) {
        if (__builtin_amdgcn_readfirstlane(fl) != 4) await(4 + idx);
        asm volatile("" ::: "memory");
        if constexpr (idx > 0) ST(8 + idx);
      }
      if constexpr (s == 0) mf0(ca[pp][0], Ba[s], A[i][0]); else mf(ca[pp][0], Ba[s], A[i][0]);
      sfence();
      if constexpr (P > 0 && s >= 2) ostep(IC<s - 2>{}, IC<pp ^ 1>{}, IC<P - 1>{});
      if constexpr (gs < 6) loadV(IC<20 + 2 * gs>{});
      if constexpr (gs >= 6 && gs < 10) swrite1(IC<1>{}, IC<2 * gs - 12>{});
      if constexpr (idx2 >= 0) fl = words[4 + idx2];
      sfence();
      if constexpr (s == 0) mf0(cgt[pp][0], Bg[s], A[i][0]); else mf(cgt[pp][0], Bg[s], A[i][0]);
      sfence();
      if constexpr (n < 64) A[i][0] = ldsA(sbase.at<sfrag((n & 63) >> 4, 0, n & 15)>());
      sfence();
      if constexpr (s == 0) mf0(cgt[pp][1], Bg[s], A[i][1]); else mf(cgt[pp][1], Bg[s], A[i][1]);
      sfence();
      if constexpr (gs < 6) loadV(IC<21 + 2 * gs>{});
      if constexpr (gs >= 6 && gs < 10) swrite1(IC<1>{}, IC<2 * gs - 11>{});
      if constexpr (gs == 11) { waitlgkm<4>(); if (lane == 0) lds_inc(misc + 5 * 4); }
      if constexpr (gs == 14) { if (lane == 0) lds_inc(misc + 9 * 4); }
      sfence();
      if constexpr (s == 0) mf0(ca[pp][1], Ba[s], A[i][1]); else mf(ca[pp][1], Ba[s], A[i][1]);
      sfence();
      if constexpr (n < 64) A[i][1] = ldsA(sbase.at<sfrag((n & 63) >> 4, 1, n & 15)>());
      sfence();
    });
    ST(12);
    nops<7>();
    sfor<0, 6>([&](auto U) { ostep(U, IC<1>{}, IC<3>{}); nops<1>(); });
    ST(13);
    STDUMP();
  } else {
    const int hw = wave - 4;
    const auto rx = __builtin_amdgcn_make_buffer_rsrc((void*)(x + (size_t)batch * (T_ * 1024)), 0, T_ * 1024, 0x00020000);
    const auto rw1 = __builtin_amdgcn_make_buffer_rsrc((void*)w1, 0, 1024 * 1024, 0x00020000);
    const int lc = (l15 >> 1) ^ q ^ ((hw >> 1) << 2);
    const int vw = (4 * hw + q) * 2048 + (lc >> 2) * 1024 + j * 128 + (lc & 3) * 32 + (lane & 1) * 16;
    const int vx = (t0 - 15 + 8 * (hw >> 1) + 2 * (hw & 1) + (lane >> 5)) * 1024 + (((((lane & 31) ^ (2 * (hw & 1) + (lane >> 5))) * 16) + KROT * 128) & 511);
    const int vx1 = vx + 32768;
    auto issue1 = [&](int n, int i) {
      if (n < 4 || (n >= 6 && n < 10)) {
        const int m = n < 4 ? n : n - 2;
        __builtin_amdgcn_raw_ptr_buffer_load_lds(rw1, smem + slot_of(n) + hw * 1024 + i * 4096, 16, vw, (((m & 4) + ((m + KROT) & 3)) * 64 + 16 * i) * 2048, 0, 0);
      } else if (n < 26) {
        const int b = n == 4 || n == 10 ? 0 : n == 5 || n == 11 ? 1 : n < 22 ? (n - 8) >> 1 : 7 + (n & 1);
        const int half = n < 6 ? 0 : n < 12 ? 1 : n < 22 ? (n & 1) : (n - 22) >> 1;
        LAS char* dst = smem + slot_of(n) + hw * 1024 + (hw >> 1) * 64 + i * (4096 + 128);
        if (b < 5) __builtin_amdgcn_raw_ptr_buffer_load_lds(rx, dst, 16, vx + (blk(b) * 32768 + half * 512 + (16 * (i & 1) + 4 * (i >> 1)) * 1024), 0, 0, 0);
        else __builtin_amdgcn_raw_ptr_buffer_load_lds(rx, dst, 16, vx1, (blk(b) - 1) * 32768 + half * 512 + (16 * (i & 1) + 4 * (i >> 1)) * 1024, 0, 0);
      } else {
        issueV(n, i);
      }
    };
    auto issue = [&](int n) {
#pragma unroll
      for (int i = 0; i < 4; i++) issue1(n, i);
      asm volatile("" ::: "memory");
    };
    STA;
    issue(0); issue(4); issue(1); issue(2); issue(3); issue(5); issue(6);
#define B0N 4
#define AFTER_B0 issue(10); issue(7);
    STA;
    bar();
    STA;
    bf16x8 T1[16], T2[8];
    auto loadT = [&](auto G) {
      LAS char* tb = smem + TAB_OFF + hw * 16 * TS;
      const int b7 = lane & 7, h1 = l15 >> 3;
      auto idx = [](int o) { return (o & 1) ? (o + 7) >> 1 : 23 + (o >> 1); };
      {
        CI(gg, G);
        LAS unsigned* p = (LAS unsigned*)opaque(gg < 4 ? tb + 4 + 4 * gg * TS + (h1 == (gg >> 1) ? idx(8 * q - b7) : 19) * 4
                                                       : tb + 4 + (8 * h1 + 4 * (gg - 4)) * TS + (q == h1 ? idx(32 - b7) : 19) * 4);
        sfor<0, 4>([&](auto QD) {
          CI(qd, QD);
          const u32x4 v = {p[qd * 44], p[qd * 44 + 1], p[qd * 44 + 2], p[qd * 44 + 3]};
          sfence();
          if constexpr (gg < 4) T1[4 * gg + qd] = __builtin_bit_cast(bf16x8, v); else T2[4 * (gg - 4) + qd] = __builtin_bit_cast(bf16x8, v);
        });
      }
    };
    sfor<0, TSPLIT>(loadT);
    STA;
    LAS char* c1 = opaque(smem + HT_OFF + hw * HREG + (l15 + q) * 16);
    LAS char* c2 = opaque(smem + HT_OFF + hw * HREG + (q & 1) * 8 * HS + (l15 + 4) * 16);
    u32x4 empty4 = {EMPTY, EMPTY, EMPTY, EMPTY};
    asm volatile("" : "+v"(empty4));
    auto mark = [&](int i) { __builtin_amdgcn_raw_buffer_store_b128(empty4, rs, svo + (i & 1) * 128, (i >> 1) * 65536, 16); asm volatile("" ::: "memory"); };
    waitvm<B0N>();
    STA;
    bar();
    STA;
    AFTER_B0
    sfor<3, 5>(loadT);
    STA;
    bar();
    STA;
    issue(8); issue(9); issue(11); issue(12);
    waitvm<8>();
    STA;
    bar();
    STA;
    issue(13);
    waitvm<8>();
    STA;
    bar();
    STA;
    issue(14);
    loadT(IC<5>{});
    waitlgkm<0>();
    waitvm<8>();
    STA;
    bar();
    STA;
    issue(15); issue(16);
    waitvm<8>();
    STA;
    bar();
    STA;
    issue(17); issue(18); mark(4);
    waitvm<9>();
    STA;
    bar();
    STA;
    issue(19); issue(20); mark(5);
    const u64 tag = *(LAS u64*)(smem + MISC_OFF + 64);
    auto lo = [] { sfence(); __builtin_amdgcn_s_setprio(0); sfence(); };
    auto hi = [] { sfence(); __builtin_amdgcn_s_setprio(3); sfence(); };
    f32x4 d[8];
    auto convmm = [&](auto HH) AI {
      CI(H, HH);
      constexpr int NW = 9;
      bf16x8 W[NW];
      auto rd = [&](auto K) { CI(k, K); constexpr int pr = k / 3, e = k % 3; W[k % NW] = ldsA((e < 2 ? c1 + (pr + 8 * e) * HS : c2 + pr * HS) + H * 256); sfence(); };
      sfor<0, NW>(rd);
      sfor<0, 24>([&](auto K) {
        CI(k, K);
        constexpr int pr = k / 3, e = k % 3;
        d[pr] = __builtin_amdgcn_mfma_f32_16x16x32_bf16(W[k % NW], e < 2 ? T1[(pr + 8 * e) & 15] : T2[pr], e == 0 ? (f32x4){0.f, 0.f, 0.f, 0.f} : d[pr], 0, 0, 0);
        sfence();
        if constexpr (k + NW < 24) rd(IC<k + NW>{});
      });
    };
    auto silu = [&](auto HH, auto R) AI {
      {
        CI(H, HH); CI(r, R);
        float t[8];
        sfor<0, 8>([&](auto I) { CI(i, I); t[i] = d[i][r] * -1.4426950408889634f; sfence(); });
        sfor<0, 8>([&](auto I) { CI(i, I); t[i] = vexp2(t[i]); });
        sfor<0, 8>([&](auto I) { CI(i, I); t[i] = vadd1(t[i]); });
        sfor<0, 8>([&](auto I) { CI(i, I); t[i] = vrcp(t[i]); });
        sfor<0, 8>([&](auto I) { CI(i, I); t[i] = vmul(t[i], d[i][r]); });
        const u32x4 v = {umin_(pk2(t[0], t[1]), EMPTY - 1), pk2(t[2], t[3]), umin_(pk2(t[4], t[5]), EMPTY - 1), pk2(t[6], t[7])};
        __builtin_amdgcn_raw_buffer_store_b128(v, rs, svo + (2 * H + (r >> 1)) * 65536 + (r & 1) * 128, 0, 16);
        asm volatile("" ::: "memory");
        sfence();
      }
    };
    const Base2 swb(smem + hw * 1024 + lane * 16);
    LAS char* swh = opaque(smem + HT_OFF + hw * HREG + lane * 16);
    auto swrite1 = [&](auto PP, auto I) {
      CI(P, PP); CI(i, I);
      if constexpr (P == 2) *(LAS u32x4*)(swh + i * 1024) = sf[0][i];
      else *(LAS u32x4*)(swb.at<sbuf(P, i >> 2) + (i & 3) * 4096>()) = sf[P & 1][i];
    };
    auto publish = [&](int P) {
      if (lane == 0) {
        if (lds_inc_rtn(misc + P * 4) == 3) __hip_atomic_store(flags + P * 128 + j * FST, tag, RLX_AGENT);
      }
    };
    waitvm<10>();
    STA;
    bar();
    STA;
    issue(21); issue(22); mark(6);
    waitvm<10>();
    STA;
    bar();
    STA;
    issue(23); issue(24); mark(7);
    waitvm<10>();
    STA;
    bar();
    STA;
    issue(25); issue(26);
    hi();
    convmm(IC<0>{}); sfor<0, 4>([&](auto R) { silu(IC<0>{}, R); });
    lo();
    waitvm<13>();
    STA;
    bar();
    STA;
    issue(27); issue(28);
    waitvm<0>();
    publish(0);
    STA;
    bar();
    STA;
    issue(29); issue(30);
    await(10);
    issue(31); issue(32); issue(33);
    waitvm<12>();
    hi();
    STA;
    bar();
    STA;
    convmm(IC<1>{});
    auto hintwait = [&](int P) {
      int po = ((4 * P + 2 * rh) * 16 + ((4 * ((lane >> 1) & 3) + hw + 2 * vrot) & 15)) * 1024 + (lane & 1) * 512 + 128;
      unsigned spins = 0;
#pragma unroll 1
      do { asm volatile("" : "+v"(po) :: "memory"); } while (__any(__builtin_amdgcn_raw_buffer_load_b32(rs, po, 0, 16) == EMPTY) && ++spins < (1u << 20));
      asm volatile("" ::: "memory");
    };
    silu(IC<1>{}, IC<0>{});
    waitvm<1>();
    if (lane == 0) lds_inc(misc + 4 * 4);
    u64 fv = flagld(0);
    silu(IC<1>{}, IC<1>{});
    STA;
    silu(IC<1>{}, IC<2>{}); silu(IC<1>{}, IC<3>{});
    HLO
    flagck(0, fv, tag);
    STA;
    if (j == 0 && threadIdx.x == 256) __hip_atomic_store(epoch, tag, RLX_AGENT);
    waitvm<0>();
    __builtin_amdgcn_s_sleep(SLPN);
    STA;
    sload(IC<2>{});
    sload(IC<3>{});
    scheck(IC<2>{});
    STA;
    sfor<0, 8>([&](auto I) { swrite1(IC<2>{}, I); });
    signal(6);
    STA;
    scheck(IC<3>{});
    await(9);
    STA;
    sfor<0, 8>([&](auto I) { swrite1(IC<3>{}, I); });
    signal(7);
    STA;
    STDUMP();
  }
}

extern "C" void kernel_launch(void* const* d_in, const int* in_sizes, int n_in, void* d_out, int out_size, void* d_ws,
                              size_t ws_size, hipStream_t stream) {
  (void)hipFuncSetAttribute((const void*)conv_module_kernel, hipFuncAttributeMaxDynamicSharedMemorySize, LDS_BYTES);
  conv_module_kernel<<<256, 512, LDS_BYTES, stream>>>((const char*)d_in[0], (const char*)d_in[1], (const char*)d_in[2],
                                                      (const char*)d_in[3], (char*)d_out, (char*)d_ws);
}
```

```cpp
#include <hip/hip_runtime.h>
#include <hip/hip_bf16.h>
typedef __hip_bfloat16 bf16;
constexpr int B_=2,T_=4000,D_=512,K_=31;
__device__ __forceinline__ float b2f(bf16 v){return __bfloat162float(v);}
__global__ void k1(const bf16* x,const bf16* w1,bf16* h){
  int c=blockIdx.x*64+threadIdx.x; int r=blockIdx.y*4+threadIdx.y;
  float a=0,g=0;
  for(int k=0;k<D_;k++){float xv=b2f(x[r*D_+k]);a+=xv*b2f(w1[k*2*D_+c]);g+=xv*b2f(w1[k*2*D_+D_+c]);}
  h[r*D_+c]=__float2bfloat16(a/(1.f+__expf(-g)));
}
__global__ void k2(const bf16* h,const bf16* dw,bf16* s){
  int c=blockIdx.x*64+threadIdx.x; int r=blockIdx.y*4+threadIdx.y; int b=r/T_,t=r%T_;
  float a=0;
  for(int i=0;i<K_;i++){int tt=t+i-15; if(tt>=0&&tt<T_) a+=b2f(h[(b*T_+tt)*D_+c])*b2f(dw[i*D_+c]);}
  s[r*D_+c]=__float2bfloat16(a/(1.f+__expf(-a)));
}
__global__ void k3(const bf16* s,const bf16* w2,bf16* o){
  int c=blockIdx.x*64+threadIdx.x; int r=blockIdx.y*4+threadIdx.y;
  float a=0;
  for(int k=0;k<D_;k++){a+=b2f(s[r*D_+k])*b2f(w2[k*D_+c]);}
  o[r*D_+c]=__float2bfloat16(a);
}
typedef unsigned long long u64;
__global__ void k0(u64* p){  if(threadIdx.x==0 && blockIdx.x==0) p[0]=__builtin_amdgcn_s_memrealtime(); }
__global__ void k4(u64* p){ u64 u=p[0]+200000; while(__builtin_amdgcn_s_memrealtime()<u) __builtin_amdgcn_s_sleep(1); }
extern "C" void kernel_launch(void* const* d_in, const int* in_sizes, int n_in,
                              void* d_out, int out_size, void* d_ws, size_t ws_size,
                              hipStream_t stream) {
  const bf16* x=(const bf16*)d_in[0]; const bf16* w1=(const bf16*)d_in[1]; const bf16* dw=(const bf16*)d_in[2]; const bf16* w2=(const bf16*)d_in[3];
  bf16* h=(bf16*)d_ws; bf16* s=h+B_*T_*D_;
  dim3 g(D_/64,B_*T_/4), b(64,4);
  u64* tp=(u64*)((char*)d_ws+(64<<20));
   k0<<<1,64,0,stream>>>(tp);
  k1<<<g,b,0,stream>>>(x,w1,h);
  k2<<<g,b,0,stream>>>(h,dw,s);
  k3<<<g,b,0,stream>>>(s,w2,(bf16*)d_out);
  k4<<<1,64,0,stream>>>(tp);
}
```

```cpp
#include <hip/hip_runtime.h>
#include <hip/hip_bf16.h>

typedef short s16x4 __attribute__((ext_vector_type(4)));
typedef __bf16 bf16x8 __attribute__((ext_vector_type(8)));
typedef __bf16 bf16x2 __attribute__((ext_vector_type(2)));
typedef float f32x4 __attribute__((ext_vector_type(4)));
typedef float f32x2 __attribute__((ext_vector_type(2)));
typedef unsigned u32x4 __attribute__((ext_vector_type(4)));
typedef unsigned u32x2 __attribute__((ext_vector_type(2)));
typedef unsigned long long u64;
#define LAS __attribute__((address_space(3)))
#define GAS __attribute__((address_space(1)))
#define DEVI __device__ __forceinline__
#define AI __attribute__((always_inline))
#define RLX_AGENT __ATOMIC_RELAXED, __HIP_MEMORY_SCOPE_AGENT

constexpr int T_ = 4000, TILE = 250;
constexpr int SLOT = 16896;
constexpr int NS = 7;
constexpr int HT_OFF = NS * SLOT;
constexpr int HS = 528;
constexpr int HREG = 16 * HS;
constexpr int TAB_OFF = HT_OFF + 2 * SLOT;
constexpr int TS = 176;
constexpr int MISC_OFF = TAB_OFF + 64 * TS;
constexpr int LDS_BYTES = 160 * 1024;
static_assert(MISC_OFF + 512 <= LDS_BYTES && HT_OFF + 64 * HS <= TAB_OFF);
constexpr size_t WS_FLAG = 8u << 20;
constexpr size_t WS_EPOCH = WS_FLAG + 32 * 4 * 8 * 128;
constexpr size_t WS_STAMP = WS_EPOCH + 32 * 128;
constexpr unsigned CODE_LINES = 200;
constexpr unsigned EMPTY = 0xFFFFFFFFu;
constexpr int PF = 3;

#define KROT gi
#define PSL 2
#define FST 16
#define S0T 1
#define HLO lo();
#define BROT 1
#define SLP
#define ORD 0
#define MARKS
#define SLPN 16
#define TSPLIT 3
#define CST
template <int N> DEVI void waitvm() { asm volatile("s_waitcnt vmcnt(%0)" ::"n"(N) : "memory"); }
template <int N> DEVI void waitlgkm() { asm volatile("s_waitcnt lgkmcnt(%0)" ::"n"(N) : "memory"); }
DEVI void bar() { asm volatile("" ::: "memory"); __builtin_amdgcn_s_barrier(); asm volatile("" ::: "memory"); }
DEVI void sfence() { __builtin_amdgcn_sched_barrier(0); }
DEVI unsigned pk2(float lo, float hi) { f32x2 p = {lo, hi}; return __builtin_bit_cast(unsigned, __builtin_convertvector(p, bf16x2)); }
DEVI bf16x8 ldsA(LAS char* p) { return *(LAS bf16x8*)p; }
DEVI s16x4 ld64(LAS char* p) { return *(LAS s16x4*)p; }
DEVI s16x4 ldtr(LAS char* p) { return __builtin_amdgcn_ds_read_tr16_b64_v4i16((LAS s16x4*)p); }
DEVI bf16x8 cat8(s16x4 a, s16x4 b) { return __builtin_bit_cast(bf16x8, __builtin_shufflevector(a, b, 0, 1, 2, 3, 4, 5, 6, 7)); }
DEVI void mf(f32x4& c, const bf16x8& a, const bf16x8& b) { asm volatile("v_mfma_f32_16x16x32_bf16 %0, %1, %2, %0" : "+v"(c) : "v"(a), "v"(b)); }
DEVI void mf0(f32x4& c, const bf16x8& a, const bf16x8& b) { asm volatile("v_mfma_f32_16x16x32_bf16 %0, %1, %2, 0" : "=&v"(c) : "v"(a), "v"(b)); }
DEVI float vnl2e(float a) { float r; asm volatile("v_mul_f32 %0, 0xbfb8aa3b, %1" : "=v"(r) : "v"(a)); return r; }
DEVI float vadd1(float a) { float r; asm volatile("v_add_f32 %0, 1.0, %1" : "=v"(r) : "v"(a)); return r; }
DEVI float vmul(float a, float b) { float r; asm volatile("v_mul_f32 %0, %1, %2" : "=v"(r) : "v"(a), "v"(b)); return r; }
DEVI float vexp2(float a) { float r; asm volatile("v_exp_f32 %0, %1" : "=v"(r) : "v"(a)); return r; }
DEVI float vrcp(float a) { float r; asm volatile("v_rcp_f32 %0, %1" : "=v"(r) : "v"(a)); return r; }
DEVI unsigned vpk(float a, float b) { unsigned r; asm volatile("v_cvt_pk_bf16_f32 %0, %1, %2" : "=v"(r) : "v"(a), "v"(b)); return r; }
DEVI LAS char* opaque(LAS char* p) { asm volatile("" : "+v"(p)); return p; }
DEVI void lds_inc(unsigned addr) { asm volatile("ds_add_u32 %0, %1" ::"v"(addr), "v"(1u) : "memory"); }
DEVI unsigned lds_inc_rtn(unsigned addr) { unsigned r; asm volatile("ds_add_rtn_u32 %0, %1, %2\n\ts_waitcnt lgkmcnt(0)" : "=&v"(r) : "v"(addr), "v"(1u) : "memory"); return r; }
DEVI unsigned umin_(unsigned a, unsigned b) { return a < b ? a : b; }
DEVI unsigned umax_(unsigned a, unsigned b) { return a > b ? a : b; }
DEVI u64 opaque64(u64 v) { asm volatile("" : "+v"(v)); return v; }
template <int N> DEVI void nops() { asm volatile("s_nop %0" ::"n"(N)); }

template <int V> struct IC { static constexpr int value = V; };
template <int B, int E, class F> DEVI void sfor(F&& f) {
  if constexpr (B < E) { f(IC<B>{}); sfor<B + 1, E>(f); }
}
#define CI(name, arg) constexpr int name = decltype(arg)::value
template <int NB> struct BaseN {
  LAS char* p[NB];
  DEVI BaseN(LAS char* b) {
    p[0] = b;
#pragma unroll
    for (int i = 1; i < NB; i++) p[i] = opaque(b + i * 61440);
  }
  template <int OFF> DEVI LAS char* at() const {
    constexpr int k = OFF / 61440 < NB ? OFF / 61440 : NB - 1;
    static_assert(OFF - k * 61440 < 65536);
    return p[k] + (OFF - k * 61440);
  }
};
typedef BaseN<2> Base2;
typedef BaseN<3> Base3;

#define ST(i) do {} while (0)
#define STA do {} while (0)
#define STR(k) do {} while (0)
#define STDUMP() do {} while (0)

constexpr int wunit(int m) { return m < 4 ? m : m + 2; }
constexpr int slot_of(int n) { return (n < 7 ? n : n < 10 ? n - 7 : n < 12 ? 18 - n : n < 31 ? (n - 2) % NS : (n - 7) % NS) * SLOT; }
constexpr int sbuf(int P, int h) { return slot_of(P == 0 ? 24 + h : P == 1 ? 29 + h : 26 + h); }
constexpr int sfrag(int P, int hf, int ks) {
  return P == 2 ? HT_OFF + (ks & 3) * HREG + (4 * (ks >> 3) + 2 * hf + ((ks >> 2) & 1)) * 1024 : sbuf(P, ks >> 3) + (hf * 8 + (ks & 7)) * 1024;
}
constexpr int PFS_OFF = MISC_OFF + 256;

__global__ __launch_bounds__(512, 2) void conv_module_kernel(const char* __restrict__ x, const char* __restrict__ w1,
                                                             const char* __restrict__ dw, const char* __restrict__ w2,
                                                             char* out, char* ws) {
  extern __shared__ __attribute__((aligned(16))) char smem_[];
  LAS char* smem = (LAS char*)smem_;
  u64 pc0;
  asm volatile("s_getpc_b64 %0" : "=s"(pc0));
  const int bid = blockIdx.x;
  const int gi = bid >> 6, j = (bid >> 3) & 7, g = (bid & 7) * 4 + gi;
  const int wave = __builtin_amdgcn_readfirstlane(threadIdx.x >> 6);
  const int lane = threadIdx.x & 63;
  const int batch = g >> 4, t0 = (g & 15) * TILE;
  const int cg = j & 3, rh = j >> 2;
  const int q = lane >> 4, l15 = lane & 15;
  const unsigned misc = (unsigned)(size_t)(smem + MISC_OFF);
  GAS u64* epoch = (GAS u64*)(ws + WS_EPOCH + g * 128);
  ST(0);
  if (wave == 1) {
    const auto rc = __builtin_amdgcn_make_buffer_rsrc((void*)pc0, 0, CODE_LINES * 128, 0x00020000);
    __builtin_amdgcn_raw_ptr_buffer_load_lds(rc, smem + PFS_OFF, 4, ((bid >> 3) * 8 + (lane & 7)) * 128, 0, 0, 0);
  }
  asm volatile("" ::: "memory");
  const int w3 = wave & 3;
  const int r5 = BROT ? (j >= 5 ? j - 5 : j) : 0, r4 = BROT ? 3 + (j >= 6 ? j - 6 : j >= 3 ? j - 3 : j) : 3, vrot = BROT ? 2 * gi + rh : 0;
  auto blk = [&](int b) { return b < 5 ? (b + r5 >= 5 ? b + r5 - 5 : b + r5) : 5 + ((b + r4) & 3); };
  const auto rx = __builtin_amdgcn_make_buffer_rsrc((void*)(x + (size_t)batch * (T_ * 1024)), 0, T_ * 1024, 0x00020000);
  const auto rw1 = __builtin_amdgcn_make_buffer_rsrc((void*)w1, 0, 1024 * 1024, 0x00020000);
  const int lc = (l15 >> 1) ^ q ^ ((w3 >> 1) << 2);
  const int vw = (4 * w3 + q) * 2048 + (lc >> 2) * 1024 + j * 128 + (lc & 3) * 32 + (lane & 1) * 16;
  const int vx = (t0 - 15 + 8 * (w3 >> 1) + 2 * (w3 & 1) + (lane >> 5)) * 1024 + (((((lane & 31) ^ (2 * (w3 & 1) + (lane >> 5))) * 16) + KROT * 128) & 511);
  const auto rdw = __builtin_amdgcn_make_buffer_rsrc((void*)dw, 0, 31 * 1024, 0x00020000);
  if (wave < 4) __builtin_amdgcn_raw_ptr_buffer_load_lds(rdw, smem + TAB_OFF + w3 * 16 * TS, 16, (lane >> 1) * 1024 + j * 128 + w3 * 32 + (lane & 1) * 16, 0, 0, 0);
  asm volatile("" ::: "memory");
  {
    const int i0 = (wave >> 2) * 2;
#pragma unroll
    for (int i = 0; i < 2; i++)
      __builtin_amdgcn_raw_ptr_buffer_load_lds(rw1, smem + slot_of(0) + w3 * 1024 + (i0 + i) * 4096, 16, vw, ((KROT & 3) * 64 + 16 * (i0 + i)) * 2048, 0, 0);
    asm volatile("" ::: "memory");
#pragma unroll
    for (int i = 0; i < 2; i++)
      __builtin_amdgcn_raw_ptr_buffer_load_lds(rx, smem + slot_of(4) + w3 * 1024 + (w3 >> 1) * 64 + (i0 + i) * (4096 + 128), 16, vx + (r5 * 32768 + (16 * i + 2 * i0) * 1024), 0, 0, 0);
    asm volatile("" ::: "memory");
  }
  ST(18);
  const auto rs = __builtin_amdgcn_make_buffer_rsrc((void*)(ws + (size_t)g * 262144), 0, 262144, 0x00020000);
  GAS u64* flags = (GAS u64*)(ws + WS_FLAG + (size_t)g * 4096);
  const int svo = (q * 16 + 2 * j + ((wave >> 1) & 1)) * 1024 + ((2 * (wave & 1) + (l15 >> 3)) * 16 + (lane & 7)) * 16;

  asm volatile("" ::: "memory");
  const auto rw2 = __builtin_amdgcn_make_buffer_rsrc((void*)w2, 0, 512 * 1024, 0x00020000);
  const int vv = (4 * w3 + q) * 1024 + cg * 256 + (((l15 >> 2) ^ q) * 64) + (lane & 3) * 16;
  auto issueV = [&](int n, int i) {
    __builtin_amdgcn_raw_ptr_buffer_load_lds(rw2, smem + slot_of(n) + w3 * 1024 + i * 4096, 16, vv, (((n - 26 + vrot) & 7) * 64 + 16 * i) * 1024, 0, 0);
  };
  u32x4 sf[2][8];
  int slo = lane * 16;
  auto sload1 = [&](auto PP, auto I) AI {
    CI(P, PP); CI(i, I);
    sf[P & 1][i] = __builtin_amdgcn_raw_buffer_load_b128(rs, slo, ((4 * P + 2 * rh + ((i & 3) >> 1)) * 16 + ((8 * (i >> 2) + 4 * (i & 1) + w3 + 2 * vrot) & 15)) * 1024, 16);
  };
  auto sload = [&](auto PP) AI { sfor<0, 8>([&](auto I) AI { sload1(PP, I); }); };
  auto flagld = [&](int P) { return __hip_atomic_load(flags + P * 128 + (lane & 7) * FST, RLX_AGENT); };
  auto flagck = [&](int P, u64 v, u64 tag) AI {
    if (!__all(v == tag)) {
      unsigned spins = 0;
#pragma unroll 1
      do { __builtin_amdgcn_s_sleep(PSL); v = flagld(P); } while (!__all(v == tag) && ++spins < (1u << 20));
    }
    asm volatile("" ::: "memory");
  };
  auto sempty = [&](auto PP) AI {
    CI(P, PP);
    unsigned m = 0;
    sfor<0, 8>([&](auto I) AI { CI(i, I); m = umax_(umax_(m, sf[P & 1][i][0]), sf[P & 1][i][1]); m = umax_(umax_(m, sf[P & 1][i][2]), sf[P & 1][i][3]); });
    return __any(m == EMPTY);
  };
  auto scheck = [&](auto PP) AI {
    if (__builtin_expect(sempty(PP), 0)) {
      unsigned spins = 0;
#pragma unroll 1
      do {
        asm volatile("" : "+v"(slo) :: "memory");
        sload(PP);
        __builtin_amdgcn_s_waitcnt(0x0F70);
      } while (sempty(PP) && ++spins < (1u << 16));
    }
  };
  auto signal = [&](int w) {
    waitlgkm<0>();
    if (lane == 0) lds_inc(misc + w * 4);
  };
  auto await = [&](int w, unsigned target = 4) {
    volatile LAS unsigned* words = (volatile LAS unsigned*)(smem + MISC_OFF);
#pragma unroll 1
    for (unsigned spins = 0; spins < (1u << 24); spins++) {
      if ((unsigned)__builtin_amdgcn_readfirstlane(words[w]) == target) break;
      __builtin_amdgcn_s_sleep(1);
    }
    asm volatile("" ::: "memory");
  };

  if (wave < 4) {
    const int ng = wave;
    __builtin_amdgcn_s_setprio(2);
    const auto rout = __builtin_amdgcn_make_buffer_rsrc((void*)(out + (size_t)(batch * T_ + t0) * 1024), 0, TILE * 1024, 0x00020000);
    waitvm<4>();
    {
      LAS char* tb = smem + TAB_OFF + ng * 16 * TS;
      unsigned tap[13];
#pragma unroll
      for (int m = 0; m < 13; m++) {
        const unsigned o = 12 * q - 9 + m;
        tap[m] = *(LAS unsigned short*)(tb + (o < 31u ? o : 31u) * 32 + l15 * 2);
      }
      waitlgkm<0>();
      LAS unsigned* tw = (LAS unsigned*)(tb + 4 + l15 * TS) + 6 * q;
#pragma unroll
      for (int m = 0; m < 12; m++) tw[(m & 1) ? 19 + (m >> 1) : (m >> 1) - 1] = tap[m] | (tap[m + 1] << 16);
      asm volatile("" ::: "memory");
      if (threadIdx.x < 16) *(LAS unsigned*)(smem + MISC_OFF + threadIdx.x * 4) = 0u;
    }
    __builtin_amdgcn_s_waitcnt(0x0F70);
    const u64 ep0 = __hip_atomic_load(epoch, RLX_AGENT);
    u64 tag = 0;
    const Base3 abase(smem + (l15 >> 1) * 1024 + (l15 >> 2) * 64 + (l15 & 1) * 512 + ((q ^ (l15 & 3)) * 16));
    constexpr int RH = 8 * 1024 + 4 * 64;
    const int q4 = l15 >> 2, p4 = lane & 3;
    const Base2 wa(smem + (8 * q + q4) * 256 + ((ng ^ q4 ^ ((q & 1) << 2)) * 32) + p4 * 8);
    const Base2 wg(smem + (8 * q + q4) * 256 + (((4 + ng) ^ q4 ^ ((q & 1) << 2)) * 32) + p4 * 8);
    LAS char* hbase = opaque(smem + HT_OFF + (ng * 16 + l15) * HS + q * 16);
    bf16x8 Ba[16], Bg[16];
    bf16x8 A[PF][2];
    f32x4 ca[3][2], cgt[3][2];
    float e[8];
    u32x4 hp, hp0;

    auto loadW = [&](auto R) {
      CI(r, R);
      constexpr int ks = r >> 1, off = slot_of(wunit(ks >> 1)) + (ks & 1) * 8192;
      if constexpr (r & 1) Bg[ks] = cat8(ldtr(wg.at<off>()), ldtr(wg.at<off + 1024>()));
      else Ba[ks] = cat8(ldtr(wa.at<off>()), ldtr(wa.at<off + 1024>()));
    };
    auto glu = [&](auto U, auto PP, auto BP) {
      CI(u0, U); CI(pp, PP); CI(bp, BP);
      constexpr int u = u0 == 0 ? 0 : u0 == 1 ? -1 : u0 - 1;
      if constexpr (u >= 0 && u < 4) { e[2 * u] = vnl2e(cgt[pp][u >> 1][(2 * u) & 3]); e[2 * u + 1] = vnl2e(cgt[pp][u >> 1][(2 * u + 1) & 3]); }
      if constexpr (u >= 4 && u < 12) e[u - 4] = vexp2(e[u - 4]);
      if constexpr (u >= 12 && u < 16) { e[2 * u - 24] = vadd1(e[2 * u - 24]); e[2 * u - 23] = vadd1(e[2 * u - 23]); }
      if constexpr (u >= 16 && u < 24) e[u - 16] = vrcp(e[u - 16]);
      if constexpr (u >= 24 && u < 28) {
        constexpr int i = 2 * u - 48;
        e[i] = vmul(e[i], ca[pp][i >> 2][i & 3]); e[i + 1] = vmul(e[i + 1], ca[pp][i >> 2][(i & 3) + 1]);
      }
      if constexpr (u == 28 || u == 29) { constexpr int i = 2 * u - 56; hp[i] = vpk(e[2 * i], e[2 * i + 1]); hp[i + 1] = vpk(e[2 * i + 2], e[2 * i + 3]); }
      if constexpr (u == 30) { if constexpr (bp == 0) hp0 = hp; else *(LAS u32x4*)(hbase + (blk(bp) & 7) * 64) = hp; }
    };
    auto step = [&](auto I, auto PP, auto KS, auto FIRST, auto NOFF, auto&& f0, auto&& f1) {
      CI(i, I); CI(pp, PP); CI(ks, KS); CI(first, FIRST); CI(noff, NOFF);
      if constexpr (first) mf0(ca[pp][0], A[i][0], Ba[ks]); else mf(ca[pp][0], A[i][0], Ba[ks]);
      sfence(); f0(); sfence();
      if constexpr (first) mf0(cgt[pp][0], A[i][0], Bg[ks]); else mf(cgt[pp][0], A[i][0], Bg[ks]);
      sfence();
      if constexpr (noff >= 0) A[i][0] = ldsA(abase.at<noff>());
      sfence();
      if constexpr (first) mf0(cgt[pp][1], A[i][1], Bg[ks]); else mf(cgt[pp][1], A[i][1], Bg[ks]);
      sfence(); f1(); sfence();
      if constexpr (first) mf0(ca[pp][1], A[i][1], Ba[ks]); else mf(ca[pp][1], A[i][1], Ba[ks]);
      sfence();
      if constexpr (noff >= 0) A[i][1] = ldsA(abase.at<noff + RH>());
      sfence();
    };
    auto nop = [] {};
    auto primeA = [&](auto N) {
      CI(n, N);
      sfor<0, PF>([&](auto I) { CI(i, I); A[i][0] = ldsA(abase.at<slot_of(n) + i * 64>()); A[i][1] = ldsA(abase.at<slot_of(n) + i * 64 + RH>()); });
    };

    waitlgkm<0>();
    ST(4);
    sfor<0, 2>([&](auto HH) {
      CI(hf, HH);
      auto aoff = [](int s) { return s < 16 ? slot_of(4 + 6 * hf + ((s >> 1) & 1)) + (2 * (s >> 2) + (s & 1)) * 64 : hf ? slot_of(12) + (s - 16) * 64 : -1; };
      sfor<0, 16>([&](auto S) {
        CI(s, S);
        if constexpr ((s & 3) == 0) {
          bar();
          if constexpr (s == 0) ST(1 + 2 * hf);
          sfor<16 * hf + s, 16 * hf + s + 4>(loadW);
          if constexpr (s == 0) sfor<0, PF>([&](auto I) { CI(i, I); A[i][0] = ldsA(abase.at<aoff(i)>()); A[i][1] = ldsA(abase.at<aoff(i) + RH>()); });
        }
        if constexpr (hf == 1 && s == 16 - PF) bar();
        step(IC<s % PF>{}, IC<(s >> 1) & 1>{}, IC<8 * hf + 2 * (s >> 2) + (s & 1)>{}, IC<hf == 0 && (s == 0 || s == 2)>{}, IC<aoff(s + PF)>{}, nop, nop);
      });
      if constexpr (hf == 0) ST(2);
    });
    ST(5); STR(0);
    const Base2 vb(smem + (8 * q + q4) * 256 + ((ng ^ q4) * 64) + p4 * 16);
    auto loadV = [&](auto R) {
      CI(r, R);
      constexpr int ks = r >> 1, off = slot_of(26 + (ks >> 1)) + (ks & 1) * 8192 + (r & 1) * 8;
      if constexpr (r & 1) Bg[ks] = cat8(ldtr(vb.at<off>()), ldtr(vb.at<off + 1024>()));
      else Ba[ks] = cat8(ldtr(vb.at<off>()), ldtr(vb.at<off + 1024>()));
    };
    sfor<2, 7>([&](auto BB) {
      CI(b, BB);
      sfor<0, 16>([&](auto S) {
        CI(s, S);
        constexpr int n = s + PF, gs = 16 * (b - 1) + s;
        if constexpr (s == 3) { waitlgkm<6>(); bar(); }
        CST
        auto fill = [&](auto G) {
          CI(gp, G);
          if constexpr (b == 2) {
            if constexpr (gp >= 1 && gp < 17) { glu(IC<2 * gp - 2>{}, IC<0>{}, IC<0>{}); glu(IC<2 * gp - 1>{}, IC<0>{}, IC<0>{}); }
            if constexpr (gp == 16) glu(IC<0>{}, IC<1>{}, IC<1>{});
            if constexpr (gp >= 17) { glu(IC<2 * gp - 33>{}, IC<1>{}, IC<1>{}); glu(IC<2 * gp - 32>{}, IC<1>{}, IC<1>{}); }
          } else {
            glu(IC<gp + 31>{}, IC<(b + 1) % 3>{}, IC<b - 2>{});
            glu(IC<gp - 1>{}, IC<(b + 2) % 3>{}, IC<b - 1>{});
          }
          if constexpr (b == 3 && gp == 2) *(LAS u32x4*)(hbase + blk(0) * 64) = hp0;
          if constexpr (b == 4 && gp == 2) { tag = opaque64(ep0) + 0x9E3779B97F4A7C15ull; if (threadIdx.x == 0) *(LAS u64*)(smem + MISC_OFF + 64) = tag; }
        };
        step(IC<gs % PF>{}, IC<b % 3>{}, IC<s>{}, IC<s == 0>{}, IC<slot_of(8 + 2 * b + (n >> 3)) + (n & 7) * 64>{},
             [&] { fill(IC<2 * s>{}); }, [&] { fill(IC<2 * s + 1>{}); });
      });
    });
    u64 fv = 0;
    sfor<0, 32>([&](auto S) {
      CI(s, S);
      constexpr int n = s + PF, gs = 96 + s, set = 1 + ((s >> 3) & 1), ks = (s >> 4) * 8 + (s & 7);
      if constexpr (s == 3 || s == 19) bar();
      if constexpr (s == 27) { waitlgkm<5>(); if (lane == 0) lds_inc(misc + 10 * 4); }
      if constexpr (s == 31) fv = flagld(0);
      auto fill = [&](auto G) {
        CI(gp, G);
        glu(IC<gp + 31>{}, IC<1>{}, IC<5>{});
        glu(IC<gp - 1>{}, IC<0>{}, IC<6>{});
        if constexpr (gp >= 38 && gp < 50) loadV(IC<gp - 38>{});
        if constexpr (gp >= 50) glu(IC<gp - 50>{}, IC<1>{}, IC<7>{});
      };
      step(IC<gs % PF>{}, IC<set>{}, IC<ks>{}, IC<ks == 0>{}, IC<(n < 32 ? slot_of(22 + (n >> 3)) + (n & 7) * 64 : -1)>{},
           [&] { fill(IC<2 * s>{}); }, [&] { fill(IC<2 * s + 1>{}); });
    });
    ST(6);
    sfor<14, 32>([&](auto U) { glu(U, IC<1>{}, IC<7>{}); });
    volatile LAS unsigned* words = (volatile LAS unsigned*)(smem + MISC_OFF);
    unsigned fl = 0;
    sfor<0, 32>([&](auto U) { glu(U, IC<2>{}, IC<8>{}); });
    waitlgkm<0>();
    ST(17);
    bar();
    ST(7); STR(1);
    const Base3 sbase(smem + lane * 16);
    const int ovo = (rh * 64 + l15) * 1024 + cg * 256 + ng * 64 + q * 16;
    unsigned op[4];
    auto ostep = [&](auto U, auto PP, auto PC) {
      CI(u, U); CI(pp, PP); CI(pc, PC);
      constexpr int hf = u / 3, s = u % 3;
      if constexpr (u < 6) {
        if constexpr (s == 0) { op[0] = vpk(ca[pp][hf][0], ca[pp][hf][1]); op[1] = vpk(ca[pp][hf][2], ca[pp][hf][3]); }
        if constexpr (s == 1) { op[2] = vpk(cgt[pp][hf][0], cgt[pp][hf][1]); op[3] = vpk(cgt[pp][hf][2], cgt[pp][hf][3]); }
        if constexpr (s == 2) __builtin_amdgcn_raw_buffer_store_b128((u32x4){op[0], op[1], op[2], op[3]}, rout, ovo + ((pc >> 1) * 128 + hf * 32 + (pc & 1) * 16) * 1024, 0, 16);
      }
    };
    const Base2 swb(smem + ng * 1024 + lane * 16);
    auto swrite1 = [&](auto PP, auto I) { CI(P, PP); CI(i, I); *(LAS u32x4*)(swb.at<sbuf(P, i >> 2) + (i & 3) * 4096>()) = sf[P & 1][i]; };
    flagck(0, fv, tag);
    sload(IC<0>{});
    sfor<12, 20>(loadV);
    ST(14);
    sfor<0, 8>([&](auto I) { swrite1(IC<0>{}, I); });
    signal(4);
    sload(IC<1>{});
    ST(15);
    await(4, 8);
    ST(8);
    sfor<0, PF>([&](auto I) { CI(i, I); A[i][0] = ldsA(sbase.at<sfrag(0, 0, i)>()); A[i][1] = ldsA(sbase.at<sfrag(0, 1, i)>()); });
    sfor<0, 64>([&](auto S) {
      CI(gs, S);
      constexpr int P = gs >> 4, s = gs & 15, i = gs % PF, pp = P & 1, n = gs + PF;
      constexpr int idx = (n == 16 || n == 32 || n == 48) ? (n >> 4) : -1;
      constexpr int n2 = n + 1, idx2 = (n2 == 16 || n2 == 32 || n2 == 48) ? (n2 >> 4) : -1;
      if constexpr (idx >= 0) {
        if (__builtin_amdgcn_readfirstlane(fl) != 4) await(4 + idx);
        asm volatile("" ::: "memory");
        if constexpr (idx > 0) ST(8 + idx);
      }
      if constexpr (s == 0) mf0(ca[pp][0], Ba[s], A[i][0]); else mf(ca[pp][0], Ba[s], A[i][0]);
      sfence();
      if constexpr (P > 0 && s >= 2) ostep(IC<s - 2>{}, IC<pp ^ 1>{}, IC<P - 1>{});
      if constexpr (gs < 6) loadV(IC<20 + 2 * gs>{});
      if constexpr (gs >= 6 && gs < 10) swrite1(IC<1>{}, IC<2 * gs - 12>{});
      if constexpr (idx2 >= 0) fl = words[4 + idx2];
      sfence();
      if constexpr (s == 0) mf0(cgt[pp][0], Bg[s], A[i][0]); else mf(cgt[pp][0], Bg[s], A[i][0]);
      sfence();
      if constexpr (n < 64) A[i][0] = ldsA(sbase.at<sfrag((n & 63) >> 4, 0, n & 15)>());
      sfence();
      if constexpr (s == 0) mf0(cgt[pp][1], Bg[s], A[i][1]); else mf(cgt[pp][1], Bg[s], A[i][1]);
      sfence();
      if constexpr (gs < 6) loadV(IC<21 + 2 * gs>{});
      if constexpr (gs >= 6 && gs < 10) swrite1(IC<1>{}, IC<2 * gs - 11>{});
      if constexpr (gs == 11) { waitlgkm<4>(); if (lane == 0) lds_inc(misc + 5 * 4); }
      if constexpr (gs == 14) { if (lane == 0) lds_inc(misc + 9 * 4); }
      sfence();
      if constexpr (s == 0) mf0(ca[pp][1], Ba[s], A[i][1]); else mf(ca[pp][1], Ba[s], A[i][1]);
      sfence();
      if constexpr (n < 64) A[i][1] = ldsA(sbase.at<sfrag((n & 63) >> 4, 1, n & 15)>());
      sfence();
    });
    ST(12);
    nops<7>();
    sfor<0, 6>([&](auto U) { ostep(U, IC<1>{}, IC<3>{}); nops<1>(); });
    ST(13);
    STDUMP();
  } else {
    const int hw = wave - 4;
    const int vx1 = vx + 32768;
    auto issue1 = [&](int n, int i) {
      if (n < 4 || (n >= 6 && n < 10)) {
        const int m = n < 4 ? n : n - 2;
        __builtin_amdgcn_raw_ptr_buffer_load_lds(rw1, smem + slot_of(n) + hw * 1024 + i * 4096, 16, vw, (((m & 4) + ((m + KROT) & 3)) * 64 + 16 * i) * 2048, 0, 0);
      } else if (n < 26) {
        const int b = n == 4 || n == 10 ? 0 : n == 5 || n == 11 ? 1 : n < 22 ? (n - 8) >> 1 : 7 + (n & 1);
        const int half = n < 6 ? 0 : n < 12 ? 1 : n < 22 ? (n & 1) : (n - 22) >> 1;
        LAS char* dst = smem + slot_of(n) + hw * 1024 + (hw >> 1) * 64 + i * (4096 + 128);
        if (b < 5) __builtin_amdgcn_raw_ptr_buffer_load_lds(rx, dst, 16, vx + (blk(b) * 32768 + half * 512 + (16 * (i & 1) + 4 * (i >> 1)) * 1024), 0, 0, 0);
        else __builtin_amdgcn_raw_ptr_buffer_load_lds(rx, dst, 16, vx1, (blk(b) - 1) * 32768 + half * 512 + (16 * (i & 1) + 4 * (i >> 1)) * 1024, 0, 0);
      } else {
        issueV(n, i);
      }
    };
    auto issue = [&](int n) {
#pragma unroll
      for (int i = 0; i < 4; i++) issue1(n, i);
      asm volatile("" ::: "memory");
    };
    STA;
    bf16x8 T1[16], T2[8];
    auto loadT = [&](auto G) {
      LAS char* tb = smem + TAB_OFF + hw * 16 * TS;
      const int b7 = lane & 7, h1 = l15 >> 3;
      auto idx = [](int o) { return (o & 1) ? (o + 7) >> 1 : 23 + (o >> 1); };
      CI(gg, G);
      LAS unsigned* p = (LAS unsigned*)opaque(tb + 4 + (8 * h1 + 4 * (gg & 1)) * TS + (gg < 2 ? idx(8 * q - b7) : q == h1 ? idx(32 - b7) : 19) * 4);
      sfor<0, 4>([&](auto QD) {
        CI(qd, QD);
        const u32x4 v = {p[qd * 44], p[qd * 44 + 1], p[qd * 44 + 2], p[qd * 44 + 3]};
        sfence();
        if constexpr (gg < 2) T1[4 * gg + qd] = __builtin_bit_cast(bf16x8, v); else T2[4 * (gg - 2) + qd] = __builtin_bit_cast(bf16x8, v);
      });
    };
    auto splitT = [&](auto PR) {
      CI(pr, PR);
      const unsigned mh = 0u - (unsigned)(l15 >> 3);
      u32x4 v = __builtin_bit_cast(u32x4, T1[pr]);
      u32x4 a = v & ~mh, b = v & mh;
      asm volatile("" : "+v"(a), "+v"(b));
      T1[pr] = __builtin_bit_cast(bf16x8, a); T1[pr + 8] = __builtin_bit_cast(bf16x8, b);
    };
    issue(5); issue(1); waitvm<4>(); STA; bar();
    issue(2); issue(3); waitvm<8>(); bar();
    issue(6); waitvm<8>(); bar();
    issue(10); waitvm<8>(); STA; bar();
    sfor<0, 4>(loadT);
    sfor<0, 8>(splitT);
    STA;
    issue(11); issue(7); waitvm<4>(); STA; bar();
    issue(8); issue(9); waitvm<8>(); bar();
    issue(12); waitvm<8>(); bar();
    issue(13); waitvm<8>(); STA; bar();
    issue(14); waitvm<8>(); STA; bar();
    u32x4 empty4 = {EMPTY, EMPTY, EMPTY, EMPTY};
    asm volatile("" : "+v"(empty4));
    auto mark = [&](int i) { __builtin_amdgcn_raw_buffer_store_b128(empty4, rs, svo + (i & 1) * 128, (i >> 1) * 65536, 16); asm volatile("" ::: "memory"); };
    issue(15); issue(16);
    waitvm<8>();
    STA;
    bar();
    STA;
    issue(17); issue(18); mark(4);
    waitvm<9>();
    STA;
    bar();
    STA;
    issue(19); issue(20); mark(5);
    u64 tag = 0;
    auto lo = [] { sfence(); __builtin_amdgcn_s_setprio(0); sfence(); };
    auto hi = [] { sfence(); __builtin_amdgcn_s_setprio(3); sfence(); };
    f32x4 d[8];
    auto convmm = [&](auto HH) AI {
      CI(H, HH);
      constexpr int NW = 9;
      bf16x8 W[NW];
      LAS char* c1 = opaque(smem + HT_OFF + hw * HREG + ((16 * H + l15 + q) & 31) * 16);
      LAS char* c2 = opaque(smem + HT_OFF + hw * HREG + (q & 1) * 8 * HS + ((16 * H + l15 + 4) & 31) * 16);
      auto rd = [&](auto K) { CI(k, K); constexpr int pr = k / 3, e = k % 3; W[k % NW] = ldsA((e < 2 ? c1 + (pr + 8 * e) * HS : c2 + pr * HS)); sfence(); };
      sfor<0, NW>(rd);
      sfor<0, 24>([&](auto K) {
        CI(k, K);
        constexpr int pr = k / 3, e = k % 3;
        d[pr] = __builtin_amdgcn_mfma_f32_16x16x32_bf16(W[k % NW], e < 2 ? T1[(pr + 8 * e) & 15] : T2[pr], e == 0 ? (f32x4){0.f, 0.f, 0.f, 0.f} : d[pr], 0, 0, 0);
        sfence();
        if constexpr (k + NW < 24) rd(IC<k + NW>{});
      });
    };
    auto silu = [&](auto HH, auto R) AI {
      {
        CI(H, HH); CI(r, R);
        float t[8];
        sfor<0, 8>([&](auto I) { CI(i, I); t[i] = d[i][r] * -1.4426950408889634f; sfence(); });
        sfor<0, 8>([&](auto I) { CI(i, I); t[i] = vexp2(t[i]); });
        sfor<0, 8>([&](auto I) { CI(i, I); t[i] = vadd1(t[i]); });
        sfor<0, 8>([&](auto I) { CI(i, I); t[i] = vrcp(t[i]); });
        sfor<0, 8>([&](auto I) { CI(i, I); t[i] = vmul(t[i], d[i][r]); });
        u32x4 v = {pk2(t[0], t[1]), pk2(t[2], t[3]), pk2(t[4], t[5]), pk2(t[6], t[7])};
        if constexpr (H == 1) v = (u32x4){umin_(v[0], EMPTY - 1), umin_(v[1], EMPTY - 1), umin_(v[2], EMPTY - 1), umin_(v[3], EMPTY - 1)};
        __builtin_amdgcn_raw_buffer_store_b128(v, rs, svo + (2 * H + (r >> 1)) * 65536 + (r & 1) * 128, 0, 16);
        asm volatile("" ::: "memory");
        sfence();
      }
    };
    const Base2 swb(smem + hw * 1024 + lane * 16);
    LAS char* swh = opaque(smem + HT_OFF + hw * HREG + lane * 16);
    auto swrite1 = [&](auto PP, auto I) {
      CI(P, PP); CI(i, I);
      if constexpr (P == 2) *(LAS u32x4*)(swh + i * 1024) = sf[0][i];
      else *(LAS u32x4*)(swb.at<sbuf(P, i >> 2) + (i & 3) * 4096>()) = sf[P & 1][i];
    };
    auto publish = [&](int P) {
      if (lane == 0) {
        if (lds_inc_rtn(misc + P * 4) == 3) __hip_atomic_store(flags + P * 128 + j * FST, tag, RLX_AGENT);
      }
    };
    waitvm<10>();
    STA;
    bar();
    STA;
    issue(21); issue(22); mark(6);
    waitvm<10>();
    STA;
    bar();
    STA;
    issue(23); issue(24); mark(7);
    waitvm<10>();
    STA;
    bar();
    STA;
    tag = *(LAS u64*)(smem + MISC_OFF + 64);
    issue(25); issue(26);
    hi();
    convmm(IC<0>{}); sfor<0, 4>([&](auto R) { silu(IC<0>{}, R); });
    lo();
    waitvm<13>();
    STA;
    bar();
    STA;
    issue(27); issue(28);
    waitvm<0>();
    publish(0);
    STA;
    bar();
    STA;
    issue(29); issue(30);
    await(10);
    issue(31); issue(32); issue(33);
    waitvm<12>();
    hi();
    STA;
    bar();
    STA;
    convmm(IC<1>{});
    silu(IC<1>{}, IC<0>{}); silu(IC<1>{}, IC<1>{});
    waitvm<2>();
    if (lane == 0) lds_inc(misc + 4 * 4);
    u64 fv = flagld(0);
    STA;
    silu(IC<1>{}, IC<2>{}); silu(IC<1>{}, IC<3>{});
    HLO
    flagck(0, fv, tag);
    STA;
    __builtin_amdgcn_s_sleep(SLPN);
    sload(IC<2>{});
    scheck(IC<2>{});
    STA;
    sfor<0, 8>([&](auto I) { swrite1(IC<2>{}, I); sload1(IC<3>{}, I); sfence(); });
    signal(6);
    STA;
    if (j == 0 && threadIdx.x == 256) __hip_atomic_store(epoch, tag, RLX_AGENT);
    scheck(IC<3>{});
    await(9);
    STA;
    sfor<0, 8>([&](auto I) { swrite1(IC<3>{}, I); });
    signal(7);
    STA;
    STDUMP();
  }
}

extern "C" void kernel_launch(void* const* d_in, const int* in_sizes, int n_in, void* d_out, int out_size, void* d_ws,
                              size_t ws_size, hipStream_t stream) {
  (void)hipFuncSetAttribute((const void*)conv_module_kernel, hipFuncAttributeMaxDynamicSharedMemorySize, LDS_BYTES);
  conv_module_kernel<<<256, 512, LDS_BYTES, stream>>>((const char*)d_in[0], (const char*)d_in[1], (const char*)d_in[2],
                                                      (const char*)d_in[3], (char*)d_out, (char*)d_ws);
}
```

```cpp
#include <hip/hip_runtime.h>
#include <hip/hip_bf16.h>

typedef short s16x4 __attribute__((ext_vector_type(4)));
typedef __bf16 bf16x8 __attribute__((ext_vector_type(8)));
typedef __bf16 bf16x2 __attribute__((ext_vector_type(2)));
typedef float f32x4 __attribute__((ext_vector_type(4)));
typedef float f32x2 __attribute__((ext_vector_type(2)));
typedef unsigned u32x4 __attribute__((ext_vector_type(4)));
typedef unsigned u32x2 __attribute__((ext_vector_type(2)));
typedef unsigned long long u64;
#define LAS __attribute__((address_space(3)))
#define GAS __attribute__((address_space(1)))
#define DEVI __device__ __forceinline__
#define AI __attribute__((always_inline))
#define RLX_AGENT __ATOMIC_RELAXED, __HIP_MEMORY_SCOPE_AGENT

constexpr int T_ = 4000, TILE = 256;
constexpr int SLOT = 16896;
constexpr int NS = 7;
constexpr int HT_OFF = NS * SLOT;
constexpr int HS = 528;
constexpr int HREG = 16 * HS;
constexpr int TAB_OFF = HT_OFF + 2 * SLOT;
constexpr int TS = 176;
constexpr int MISC_OFF = TAB_OFF + 64 * TS;
constexpr int LDS_BYTES = 160 * 1024;
static_assert(MISC_OFF + 512 <= LDS_BYTES && HT_OFF + 64 * HS <= TAB_OFF);
constexpr size_t WS_FLAG = 8u << 20;
constexpr size_t WS_EPOCH = WS_FLAG + 32 * 4 * 8 * 128;
constexpr size_t WS_HEPOCH = WS_EPOCH + 32 * 128;
constexpr size_t WS_HFLAG = WS_HEPOCH + 264 * 128;
constexpr size_t WS_HALO = WS_HFLAG + 264 * 128;
constexpr size_t WS_STAMP = WS_HALO + 264 * 4096;
constexpr unsigned CODE_LINES = 184;
constexpr unsigned EMPTY = 0xFFFFFFFFu;
constexpr int PF = 3;

#define KROT gi
#define PSL 2
#define FST 16
#define S0T 1
#define HLO lo();
#define BROT 1
#define SLP
#define ORD 0
#define MARKS
#define SLPN 16
#define TSPLIT 3
#define OAUX 2
#define EARLY 0
#define CSLPN 28
#define CST
template <int N> DEVI void waitvm() { asm volatile("s_waitcnt vmcnt(%0)" ::"n"(N) : "memory"); }
template <int N> DEVI void waitlgkm() { asm volatile("s_waitcnt lgkmcnt(%0)" ::"n"(N) : "memory"); }
DEVI void bar() { asm volatile("" ::: "memory"); __builtin_amdgcn_s_barrier(); asm volatile("" ::: "memory"); }
DEVI void sfence() { __builtin_amdgcn_sched_barrier(0); }
DEVI unsigned pk2(float lo, float hi) { f32x2 p = {lo, hi}; return __builtin_bit_cast(unsigned, __builtin_convertvector(p, bf16x2)); }
DEVI bf16x8 ldsA(LAS char* p) { return *(LAS bf16x8*)p; }
DEVI s16x4 ld64(LAS char* p) { return *(LAS s16x4*)p; }
DEVI s16x4 ldtr(LAS char* p) { return __builtin_amdgcn_ds_read_tr16_b64_v4i16((LAS s16x4*)p); }
DEVI bf16x8 cat8(s16x4 a, s16x4 b) { return __builtin_bit_cast(bf16x8, __builtin_shufflevector(a, b, 0, 1, 2, 3, 4, 5, 6, 7)); }
DEVI void mf(f32x4& c, const bf16x8& a, const bf16x8& b) { asm volatile("v_mfma_f32_16x16x32_bf16 %0, %1, %2, %0" : "+v"(c) : "v"(a), "v"(b)); }
DEVI void mf0(f32x4& c, const bf16x8& a, const bf16x8& b) { asm volatile("v_mfma_f32_16x16x32_bf16 %0, %1, %2, 0" : "=&v"(c) : "v"(a), "v"(b)); }
DEVI float vnl2e(float a) { float r; asm volatile("v_mul_f32 %0, 0xbfb8aa3b, %1" : "=v"(r) : "v"(a)); return r; }
DEVI float vadd1(float a) { float r; asm volatile("v_add_f32 %0, 1.0, %1" : "=v"(r) : "v"(a)); return r; }
DEVI float vmul(float a, float b) { float r; asm volatile("v_mul_f32 %0, %1, %2" : "=v"(r) : "v"(a), "v"(b)); return r; }
DEVI float vexp2(float a) { float r; asm volatile("v_exp_f32 %0, %1" : "=v"(r) : "v"(a)); return r; }
DEVI float vrcp(float a) { float r; asm volatile("v_rcp_f32 %0, %1" : "=v"(r) : "v"(a)); return r; }
DEVI unsigned vpk(float a, float b) { unsigned r; asm volatile("v_cvt_pk_bf16_f32 %0, %1, %2" : "=v"(r) : "v"(a), "v"(b)); return r; }
DEVI LAS char* opaque(LAS char* p) { asm volatile("" : "+v"(p)); return p; }
DEVI void lds_inc(unsigned addr) { asm volatile("ds_add_u32 %0, %1" ::"v"(addr), "v"(1u) : "memory"); }
DEVI unsigned lds_inc_rtn(unsigned addr) { unsigned r; asm volatile("ds_add_rtn_u32 %0, %1, %2\n\ts_waitcnt lgkmcnt(0)" : "=&v"(r) : "v"(addr), "v"(1u) : "memory"); return r; }
DEVI unsigned umin_(unsigned a, unsigned b) { return a < b ? a : b; }
DEVI unsigned umax_(unsigned a, unsigned b) { return a > b ? a : b; }
DEVI u64 opaque64(u64 v) { asm volatile("" : "+v"(v)); return v; }
DEVI u64 rfl64(u64 v) { return (u64)(unsigned)__builtin_amdgcn_readfirstlane((int)v) | ((u64)(unsigned)__builtin_amdgcn_readfirstlane((int)(v >> 32)) << 32); }
template <int N> DEVI void nops() { asm volatile("s_nop %0" ::"n"(N)); }

template <int V> struct IC { static constexpr int value = V; };
template <int B, int E, class F> DEVI void sfor(F&& f) {
  if constexpr (B < E) { f(IC<B>{}); sfor<B + 1, E>(f); }
}
#define CI(name, arg) constexpr int name = decltype(arg)::value
template <int NB> struct BaseN {
  LAS char* p[NB];
  DEVI BaseN(LAS char* b) {
    p[0] = b;
#pragma unroll
    for (int i = 1; i < NB; i++) p[i] = opaque(b + i * 61440);
  }
  template <int OFF> DEVI LAS char* at() const {
    constexpr int k = OFF / 61440 < NB ? OFF / 61440 : NB - 1;
    static_assert(OFF - k * 61440 < 65536);
    return p[k] + (OFF - k * 61440);
  }
};
typedef BaseN<2> Base2;
typedef BaseN<3> Base3;

#define ST(i) do {} while (0)
#define STA do {} while (0)
#define STR(k) do {} while (0)
#define STDUMP() do {} while (0)

constexpr int wunit(int m) { return m < 4 ? m : m + 2; }
constexpr int slot_of(int n) { return (n < 7 ? n : n < 10 ? n - 7 : n < 12 ? 18 - n : (n - 2) % NS) * SLOT; }
constexpr int sbuf(int P, int h) { return slot_of(P == 0 ? 25 + h : P == 1 ? 27 + h : 29 + h); }
constexpr int sfrag(int P, int hf, int ks) {
  return P == 2 ? HT_OFF + (ks & 3) * HREG + (4 * (ks >> 3) + 2 * hf + ((ks >> 2) & 1)) * 1024 : sbuf(P, ks >> 3) + (hf * 8 + (ks & 7)) * 1024;
}
constexpr int PFS_OFF = MISC_OFF + 256;

__global__ __launch_bounds__(512, 2) void conv_module_kernel(const char* __restrict__ x, const char* __restrict__ w1,
                                                             const char* __restrict__ dw, const char* __restrict__ w2,
                                                             char* out, char* ws) {
  extern __shared__ __attribute__((aligned(16))) char smem_[];
  LAS char* smem = (LAS char*)smem_;
  u64 pc0;
  asm volatile("s_getpc_b64 %0" : "=s"(pc0));
  const int bid = blockIdx.x;
  const int gi = bid >> 6, j = (bid >> 3) & 7, g = (bid & 7) * 4 + gi;
  const int wave = __builtin_amdgcn_readfirstlane(threadIdx.x >> 6);
  const int lane = threadIdx.x & 63;
  const int batch = g >> 4, t0 = (g & 15) * TILE;
  const int cg = j & 3, rh = j >> 2;
  const int q = lane >> 4, l15 = lane & 15;
  const unsigned misc = (unsigned)(size_t)(smem + MISC_OFF);
  GAS u64* epoch = (GAS u64*)(ws + WS_EPOCH + g * 128);
  ST(0);
  if (wave == 1) {
    const auto rc = __builtin_amdgcn_make_buffer_rsrc((void*)pc0, 0, CODE_LINES * 128, 0x00020000);
    __builtin_amdgcn_raw_ptr_buffer_load_lds(rc, smem + PFS_OFF, 4, ((bid >> 3) * 8 + (lane & 7)) * 128, 0, 0, 0);
  }
  asm volatile("" ::: "memory");
  const int w3 = wave & 3;
  const u64 ordp = (j & 4) ? ((j & 2) ? 0x1325478632147685ull : 0x2137658413276548ull) : ((j & 2) ? 0x3216548721354786ull : 0x1324768532176584ull);
  const unsigned ord = (unsigned)(ordp >> (32 * (j & 1)));
  const int vrot = 2 * gi + rh;
  auto blk = [&](int b) { return (int)((ord >> (4 * b)) & 15); };
  const auto rx = __builtin_amdgcn_make_buffer_rsrc((void*)(x + (size_t)batch * (T_ * 1024)), 0, T_ * 1024, 0x00020000);
  const auto rw1 = __builtin_amdgcn_make_buffer_rsrc((void*)w1, 0, 1024 * 1024, 0x00020000);
  const int lc = (l15 >> 1) ^ q ^ ((w3 >> 1) << 2);
  const int vw = (4 * w3 + q) * 2048 + (lc >> 2) * 1024 + j * 128 + (lc & 3) * 32 + (lane & 1) * 16;
  const int vx = (t0 + 8 * (w3 >> 1) + 2 * (w3 & 1) + (lane >> 5)) * 1024 + (((((lane & 31) ^ (2 * (w3 & 1) + (lane >> 5))) * 16) + KROT * 128) & 511);
  const auto rdw = __builtin_amdgcn_make_buffer_rsrc((void*)dw, 0, 31 * 1024, 0x00020000);
  if (wave < 4) __builtin_amdgcn_raw_ptr_buffer_load_lds(rdw, smem + TAB_OFF + w3 * 16 * TS, 16, (lane >> 1) * 1024 + j * 128 + w3 * 32 + (lane & 1) * 16, 0, 0, 0);
  asm volatile("" ::: "memory");
  {
    const int i0 = (wave >> 2) * 2;
#pragma unroll
    for (int i = 0; i < 2; i++)
      __builtin_amdgcn_raw_ptr_buffer_load_lds(rw1, smem + slot_of(0) + w3 * 1024 + (i0 + i) * 4096, 16, vw, ((KROT & 3) * 64 + 16 * (i0 + i)) * 2048, 0, 0);
    asm volatile("" ::: "memory");
#pragma unroll
    for (int i = 0; i < 2; i++)
      __builtin_amdgcn_raw_ptr_buffer_load_lds(rx, smem + slot_of(4) + w3 * 1024 + (w3 >> 1) * 64 + (i0 + i) * (4096 + 128), 16, vx, (blk(0) - 1) * 32768 + (16 * i + 2 * i0) * 1024, 0, 0);
    asm volatile("" ::: "memory");
  }
  ST(18);
  const auto rs = __builtin_amdgcn_make_buffer_rsrc((void*)(ws + (size_t)g * 262144), 0, 262144, 0x00020000);
  GAS u64* flags = (GAS u64*)(ws + WS_FLAG + (size_t)g * 4096);
  const int svo = (q * 16 + 2 * j + ((wave >> 1) & 1)) * 1024 + ((2 * (wave & 1) + (l15 >> 3)) * 16 + (lane & 7)) * 16;

  asm volatile("" ::: "memory");
  const auto rw2 = __builtin_amdgcn_make_buffer_rsrc((void*)w2, 0, 512 * 1024, 0x00020000);
  const int vv = (4 * w3 + q) * 1024 + cg * 256 + (((l15 >> 2) ^ q) * 64) + (lane & 3) * 16;
  auto issueV = [&](int n, int i) {
    __builtin_amdgcn_raw_ptr_buffer_load_lds(rw2, smem + slot_of(n) + w3 * 1024 + i * 4096, 16, vv, (((n - 24 + vrot) & 7) * 64 + 16 * i) * 1024, 0, 0);
  };
  u32x4 sf[2][8];
  int slo = lane * 16;
  auto sload1 = [&](auto PP, auto I) AI {
    CI(P, PP); CI(i, I);
    sf[P & 1][i] = __builtin_amdgcn_raw_buffer_load_b128(rs, slo, ((4 * P + 2 * rh + ((i & 3) >> 1)) * 16 + ((8 * (i >> 2) + 4 * (i & 1) + w3 + 2 * vrot) & 15)) * 1024, 16);
  };
  auto sload = [&](auto PP) AI { sfor<0, 8>([&](auto I) AI { sload1(PP, I); }); };
  auto sempty = [&](auto PP) AI {
    CI(P, PP);
    unsigned m = 0;
    sfor<0, 8>([&](auto I) AI { CI(i, I); m = umax_(umax_(m, sf[P & 1][i][0]), sf[P & 1][i][1]); m = umax_(umax_(m, sf[P & 1][i][2]), sf[P & 1][i][3]); });
    return __any(m == EMPTY);
  };
  auto scheck = [&](auto PP) AI {
    if (__builtin_expect(sempty(PP), 0)) {
      unsigned spins = 0;
#pragma unroll 1
      do {
        asm volatile("" : "+v"(slo) :: "memory");
        sload(PP);
        __builtin_amdgcn_s_waitcnt(0x0F70);
      } while (sempty(PP) && ++spins < (1u << 16));
    }
  };
  auto signal = [&](int w) {
    waitlgkm<0>();
    if (lane == 0) lds_inc(misc + w * 4);
  };
  auto await = [&](int w, unsigned target = 4) {
    volatile LAS unsigned* words = (volatile LAS unsigned*)(smem + MISC_OFF);
#pragma unroll 1
    for (unsigned spins = 0; spins < (1u << 24); spins++) {
      if ((unsigned)__builtin_amdgcn_readfirstlane(words[w]) == target) break;
      __builtin_amdgcn_s_sleep(1);
    }
    asm volatile("" ::: "memory");
  };

  if (wave < 4) {
    const int ng = wave;
    __builtin_amdgcn_s_setprio(2);
    const auto rout = __builtin_amdgcn_make_buffer_rsrc((void*)(out + (size_t)batch * (T_ * 1024)), 0, T_ * 1024, 0x00020000);
    waitvm<4>();
    {
      LAS char* tb = smem + TAB_OFF + ng * 16 * TS;
      unsigned tap[13];
#pragma unroll
      for (int m = 0; m < 13; m++) {
        const unsigned o = 12 * q - 9 + m;
        tap[m] = *(LAS unsigned short*)(tb + (o < 31u ? o : 31u) * 32 + l15 * 2);
      }
      waitlgkm<0>();
      LAS unsigned* tw = (LAS unsigned*)(tb + 4 + l15 * TS) + 6 * q;
#pragma unroll
      for (int m = 0; m < 12; m++) tw[(m & 1) ? 19 + (m >> 1) : (m >> 1) - 1] = tap[m] | (tap[m + 1] << 16);
      asm volatile("" ::: "memory");
      if (threadIdx.x < 16) *(LAS unsigned*)(smem + MISC_OFF + threadIdx.x * 4) = 0u;
    }
    __builtin_amdgcn_s_waitcnt(0x0F70);
    const u64 ep0 = __hip_atomic_load(lane == 0 ? epoch : (GAS u64*)(ws + WS_HEPOCH + (g * 8 + j + (lane == 2 ? 8 : 0)) * 128), RLX_AGENT);
    const Base3 abase(smem + (l15 >> 1) * 1024 + (l15 >> 2) * 64 + (l15 & 1) * 512 + ((q ^ (l15 & 3)) * 16));
    constexpr int RH = 8 * 1024 + 4 * 64;
    const int q4 = l15 >> 2, p4 = lane & 3;
    const Base2 wa(smem + (8 * q + q4) * 256 + ((ng ^ q4 ^ ((q & 1) << 2)) * 32) + p4 * 8);
    const Base2 wg(smem + (8 * q + q4) * 256 + (((4 + ng) ^ q4 ^ ((q & 1) << 2)) * 32) + p4 * 8);
    LAS char* hbase = opaque(smem + HT_OFF + (ng * 16 + l15) * HS + q * 16);
    bf16x8 Ba[16], Bg[16];
    bf16x8 A[PF][2];
    f32x4 ca[3][2], cgt[3][2];
    float e[8];
    u32x4 hp, hp0;

    auto loadW = [&](auto R) {
      CI(r, R);
      constexpr int ks = r >> 1, off = slot_of(wunit(ks >> 1)) + (ks & 1) * 8192;
      if constexpr (r & 1) Bg[ks] = cat8(ldtr(wg.at<off>()), ldtr(wg.at<off + 1024>()));
      else Ba[ks] = cat8(ldtr(wa.at<off>()), ldtr(wa.at<off + 1024>()));
    };
    auto glu = [&](auto U, auto PP, auto BP) {
      CI(u0, U); CI(pp, PP); CI(bp, BP);
      constexpr int u = u0 == 0 ? 0 : u0 == 1 ? -1 : u0 - 1;
      if constexpr (u >= 0 && u < 4) { e[2 * u] = vnl2e(cgt[pp][u >> 1][(2 * u) & 3]); e[2 * u + 1] = vnl2e(cgt[pp][u >> 1][(2 * u + 1) & 3]); }
      if constexpr (u >= 4 && u < 12) e[u - 4] = vexp2(e[u - 4]);
      if constexpr (u >= 12 && u < 16) { e[2 * u - 24] = vadd1(e[2 * u - 24]); e[2 * u - 23] = vadd1(e[2 * u - 23]); }
      if constexpr (u >= 16 && u < 24) e[u - 16] = vrcp(e[u - 16]);
      if constexpr (u >= 24 && u < 28) {
        constexpr int i = 2 * u - 48;
        e[i] = vmul(e[i], ca[pp][i >> 2][i & 3]); e[i + 1] = vmul(e[i + 1], ca[pp][i >> 2][(i & 3) + 1]);
      }
      if constexpr (u == 28 || u == 29) { constexpr int i = 2 * u - 56; hp[i] = vpk(e[2 * i], e[2 * i + 1]); hp[i + 1] = vpk(e[2 * i + 2], e[2 * i + 3]); }
      if constexpr (u == 30) { if constexpr (bp == 0) hp0 = hp; else *(LAS u32x4*)(hbase + (blk(bp) & 7) * 64) = hp; }
    };
    auto step = [&](auto I, auto PP, auto KS, auto FIRST, auto NOFF, auto&& f0, auto&& f1) {
      CI(i, I); CI(pp, PP); CI(ks, KS); CI(first, FIRST); CI(noff, NOFF);
      if constexpr (first) mf0(ca[pp][0], A[i][0], Ba[ks]); else mf(ca[pp][0], A[i][0], Ba[ks]);
      sfence(); f0(); sfence();
      if constexpr (first) mf0(cgt[pp][0], A[i][0], Bg[ks]); else mf(cgt[pp][0], A[i][0], Bg[ks]);
      sfence();
      if constexpr (noff >= 0) A[i][0] = ldsA(abase.at<noff>());
      sfence();
      if constexpr (first) mf0(cgt[pp][1], A[i][1], Bg[ks]); else mf(cgt[pp][1], A[i][1], Bg[ks]);
      sfence(); f1(); sfence();
      if constexpr (first) mf0(ca[pp][1], A[i][1], Ba[ks]); else mf(ca[pp][1], A[i][1], Ba[ks]);
      sfence();
      if constexpr (noff >= 0) A[i][1] = ldsA(abase.at<noff + RH>());
      sfence();
    };
    auto nop = [] {};
    auto primeA = [&](auto N) {
      CI(n, N);
      sfor<0, PF>([&](auto I) { CI(i, I); A[i][0] = ldsA(abase.at<slot_of(n) + i * 64>()); A[i][1] = ldsA(abase.at<slot_of(n) + i * 64 + RH>()); });
    };

    waitlgkm<0>();
    ST(4);
    sfor<0, 2>([&](auto HH) {
      CI(hf, HH);
      auto aoff = [](int s) { return s < 16 ? slot_of(4 + 6 * hf + ((s >> 1) & 1)) + (2 * (s >> 2) + (s & 1)) * 64 : hf ? slot_of(12) + (s - 16) * 64 : -1; };
      sfor<0, 16>([&](auto S) {
        CI(s, S);
        if constexpr ((s & 3) == 0) {
          bar();
          if constexpr (s == 0) ST(1 + 2 * hf);
          sfor<16 * hf + s, 16 * hf + s + 4>(loadW);
          if constexpr (s == 0) sfor<0, PF>([&](auto I) { CI(i, I); A[i][0] = ldsA(abase.at<aoff(i)>()); A[i][1] = ldsA(abase.at<aoff(i) + RH>()); });
        }
        if constexpr (hf == 1 && s == 16 - PF) bar();
        step(IC<s % PF>{}, IC<(s >> 1) & 1>{}, IC<8 * hf + 2 * (s >> 2) + (s & 1)>{}, IC<hf == 0 && (s == 0 || s == 2)>{}, IC<aoff(s + PF)>{}, nop, nop);
      });
      if constexpr (hf == 0) ST(2);
    });
    ST(5); STR(0);
    const Base2 vb(smem + (8 * q + q4) * 256 + ((ng ^ q4) * 64) + p4 * 16);
    auto loadV = [&](auto R) {
      CI(r, R);
      constexpr int ks = r >> 1, off = slot_of(24 + (ks >> 1)) + (ks & 1) * 8192 + (r & 1) * 8;
      if constexpr (r & 1) Bg[ks] = cat8(ldtr(vb.at<off>()), ldtr(vb.at<off + 1024>()));
      else Ba[ks] = cat8(ldtr(vb.at<off>()), ldtr(vb.at<off + 1024>()));
    };
    sfor<2, 8>([&](auto BB) {
      CI(b, BB);
      sfor<0, 16>([&](auto S) {
        CI(s, S);
        constexpr int n = s + PF, gs = 16 * (b - 1) + s;
        if constexpr (s == 3) { waitlgkm<6>(); bar(); }
        CST
        auto fill = [&](auto G) {
          CI(gp, G);
          if constexpr (b == 2) {
            if constexpr (gp >= 1 && gp < 17) { glu(IC<2 * gp - 2>{}, IC<0>{}, IC<0>{}); glu(IC<2 * gp - 1>{}, IC<0>{}, IC<0>{}); }
            if constexpr (gp == 16) glu(IC<0>{}, IC<1>{}, IC<1>{});
            if constexpr (gp >= 17) { glu(IC<2 * gp - 33>{}, IC<1>{}, IC<1>{}); glu(IC<2 * gp - 32>{}, IC<1>{}, IC<1>{}); }
          } else {
            glu(IC<gp + 31>{}, IC<(b + 1) % 3>{}, IC<b - 2>{});
            glu(IC<gp - 1>{}, IC<(b + 2) % 3>{}, IC<b - 1>{});
          }
          if constexpr (b == 3 && gp == 2) *(LAS u32x4*)(hbase + (blk(0) & 7) * 64) = hp0;
          if constexpr (b == 2 && gp == 0) {
            const u64 tg = opaque64(ep0) + 0x9E3779B97F4A7C15ull;
            if (threadIdx.x < 3) *(LAS u64*)(smem + MISC_OFF + 64 + threadIdx.x * 8) = tg;
          }
        };
        step(IC<gs % PF>{}, IC<b % 3>{}, IC<s>{}, IC<s == 0>{}, IC<(b < 7 || n < 16) ? slot_of(8 + 2 * b + (n >> 3)) + (n & 7) * 64 : -1>{},
             [&] { fill(IC<2 * s>{}); }, [&] { fill(IC<2 * s + 1>{}); });
      });
    });
    ST(6);
    glu(IC<31>{}, IC<0>{}, IC<6>{});
    nops<7>();
    sfor<0, 32>([&](auto U) { glu(U, IC<1>{}, IC<7>{}); });
    volatile LAS unsigned* words = (volatile LAS unsigned*)(smem + MISC_OFF);
    unsigned fl = 0;
    waitlgkm<0>();
    ST(17);
    bar();
    ST(7); STR(1);
    const Base3 sbase(smem + lane * 16);
    const int ovo = (t0 - 15 + rh * 64 + l15) * 1024 + cg * 256 + ng * 64 + q * 16;
    unsigned op[4];
    auto ostep = [&](auto U, auto PP, auto PC) {
      CI(u, U); CI(pp, PP); CI(pc, PC);
      constexpr int hf = u / 3, s = u % 3;
      if constexpr (u < 6) {
        if constexpr (s == 0) { op[0] = vpk(ca[pp][hf][0], ca[pp][hf][1]); op[1] = vpk(ca[pp][hf][2], ca[pp][hf][3]); }
        if constexpr (s == 1) { op[2] = vpk(cgt[pp][hf][0], cgt[pp][hf][1]); op[3] = vpk(cgt[pp][hf][2], cgt[pp][hf][3]); }
        if constexpr (s == 2) __builtin_amdgcn_raw_buffer_store_b128((u32x4){op[0], op[1], op[2], op[3]}, rout, ovo + ((1 - (pc >> 1)) * 128 + hf * 32 + (pc & 1) * 16) * 1024, 0, OAUX);
      }
    };
    const Base2 swb(smem + ng * 1024 + lane * 16);
    auto swrite1 = [&](auto PP, auto I) { CI(P, PP); CI(i, I); *(LAS u32x4*)(swb.at<sbuf(P, i >> 2) + (i & 3) * 4096>()) = sf[P & 1][i]; };
    sfor<0, 20>(loadV);
    sfence();
    sfor<0, 8>([&](auto I) { CI(i, I); issueV(29 + (i >> 2), i & 3); });
    signal(3);
    await(3);
    sfor<0, 4>([&](auto I) { CI(i, I); issueV(31, i); });
    ST(14);
    await(1);
    __builtin_amdgcn_s_sleep(CSLPN);
    sload(IC<0>{});
    scheck(IC<0>{});
    ST(15);
    sfor<0, 8>([&](auto I) { swrite1(IC<0>{}, I); sload1(IC<1>{}, I); sfence(); });
    signal(4);
    await(4);
    ST(8);
    sfor<0, PF>([&](auto I) { CI(i, I); A[i][0] = ldsA(sbase.at<sfrag(0, 0, i)>()); A[i][1] = ldsA(sbase.at<sfrag(0, 1, i)>()); });
    sfor<0, 64>([&](auto S) {
      CI(gs, S);
      constexpr int P = gs >> 4, s = gs & 15, i = gs % PF, pp = P & 1, n = gs + PF;
      constexpr int idx = (n == 16 || n == 32 || n == 48) ? (n >> 4) : -1;
      constexpr int n2 = n + 1, idx2 = (n2 == 16 || n2 == 32 || n2 == 48) ? (n2 >> 4) : -1;
      if constexpr (idx >= 0) {
        if (__builtin_amdgcn_readfirstlane(fl) != 4) await(4 + idx);
        asm volatile("" ::: "memory");
        if constexpr (idx > 0) ST(8 + idx);
      }
      if constexpr (gs == 6) scheck(IC<1>{});
      if constexpr (s == 0) mf0(ca[pp][0], Ba[s], A[i][0]); else mf(ca[pp][0], Ba[s], A[i][0]);
      sfence();
      if constexpr (P > 0 && s >= 2) ostep(IC<s - 2>{}, IC<pp ^ 1>{}, IC<P - 1>{});
      if constexpr (gs < 6) loadV(IC<20 + 2 * gs>{});
      if constexpr (gs >= 6 && gs < 10) swrite1(IC<1>{}, IC<2 * gs - 12>{});
      if constexpr (idx2 >= 0) fl = words[4 + idx2];
      sfence();
      if constexpr (s == 0) mf0(cgt[pp][0], Bg[s], A[i][0]); else mf(cgt[pp][0], Bg[s], A[i][0]);
      sfence();
      if constexpr (n < 64) A[i][0] = ldsA(sbase.at<sfrag((n & 63) >> 4, 0, n & 15)>());
      sfence();
      if constexpr (s == 0) mf0(cgt[pp][1], Bg[s], A[i][1]); else mf(cgt[pp][1], Bg[s], A[i][1]);
      sfence();
      if constexpr (gs < 6) loadV(IC<21 + 2 * gs>{});
      if constexpr (gs >= 6 && gs < 10) swrite1(IC<1>{}, IC<2 * gs - 11>{});
      if constexpr (gs == 14) { if (lane == 0) lds_inc(misc + 9 * 4); }
      if constexpr (gs == 11) { waitlgkm<4>(); if (lane == 0) lds_inc(misc + 5 * 4); }
      sfence();
      if constexpr (s == 0) mf0(ca[pp][1], Ba[s], A[i][1]); else mf(ca[pp][1], Ba[s], A[i][1]);
      sfence();
      if constexpr (n < 64) A[i][1] = ldsA(sbase.at<sfrag((n & 63) >> 4, 1, n & 15)>());
      sfence();
    });
    ST(12);
    nops<7>();
    sfor<0, 6>([&](auto U) { ostep(U, IC<1>{}, IC<3>{}); nops<1>(); });
    ST(13);
    STDUMP();
  } else {
    const int hw = wave - 4;
    auto issue1 = [&](int n, int i) {
      if (n < 4 || (n >= 6 && n < 10)) {
        const int m = n < 4 ? n : n - 2;
        __builtin_amdgcn_raw_ptr_buffer_load_lds(rw1, smem + slot_of(n) + hw * 1024 + i * 4096, 16, vw, (((m & 4) + ((m + KROT) & 3)) * 64 + 16 * i) * 2048, 0, 0);
      } else if (n < 24) {
        const int b = n == 4 || n == 10 ? 0 : n == 5 || n == 11 ? 1 : (n - 8) >> 1;
        const int half = n < 6 ? 0 : n < 12 ? 1 : (n & 1);
        __builtin_amdgcn_raw_ptr_buffer_load_lds(rx, smem + slot_of(n) + hw * 1024 + (hw >> 1) * 64 + i * (4096 + 128), 16, vx,
                                                 (blk(b) - 1) * 32768 + half * 512 + (16 * (i & 1) + 4 * (i >> 1)) * 1024, 0, 0);
      } else {
        issueV(n, i);
      }
    };
    auto issue = [&](int n) {
#pragma unroll
      for (int i = 0; i < 4; i++) issue1(n, i);
      asm volatile("" ::: "memory");
    };
    STA;
    bf16x8 T1[16], T2[8];
    auto loadT = [&](auto G) {
      LAS char* tb = smem + TAB_OFF + hw * 16 * TS;
      const int b7 = lane & 7, h1 = l15 >> 3;
      auto idx = [](int o) { return (o & 1) ? (o + 7) >> 1 : 23 + (o >> 1); };
      CI(gg, G);
      LAS unsigned* p = (LAS unsigned*)opaque(tb + 4 + (8 * h1 + 4 * (gg & 1)) * TS + (gg < 2 ? idx(8 * q - b7 - 2) : q == h1 ? idx(30 - b7) : 19) * 4);
      sfor<0, 4>([&](auto QD) {
        CI(qd, QD);
        const u32x4 v = {p[qd * 44], p[qd * 44 + 1], p[qd * 44 + 2], p[qd * 44 + 3]};
        sfence();
        if constexpr (gg < 2) T1[4 * gg + qd] = __builtin_bit_cast(bf16x8, v); else T2[4 * (gg - 2) + qd] = __builtin_bit_cast(bf16x8, v);
      });
    };
    auto splitT = [&](auto PR) {
      CI(pr, PR);
      const unsigned mh = 0u - (unsigned)(l15 >> 3);
      u32x4 v = __builtin_bit_cast(u32x4, T1[pr]);
      u32x4 a = v & ~mh, b = v & mh;
      asm volatile("" : "+v"(a), "+v"(b));
      T1[pr] = __builtin_bit_cast(bf16x8, a); T1[pr + 8] = __builtin_bit_cast(bf16x8, b);
    };
    issue(5); issue(1); waitvm<4>(); STA; bar();
    issue(2); issue(3); waitvm<8>(); bar();
    issue(6); waitvm<8>(); bar();
    issue(10); waitvm<8>(); STA; bar();
    sfor<0, 4>(loadT);
    sfor<0, 8>(splitT);
    STA;
    issue(11); issue(7); waitvm<4>(); STA; bar();
    issue(8); issue(9); waitvm<8>(); bar();
    issue(12); waitvm<8>(); bar();
    issue(13); waitvm<8>(); STA; bar();
    issue(14); waitvm<8>(); STA; bar();
    u32x4 empty4 = {EMPTY, EMPTY, EMPTY, EMPTY};
    asm volatile("" : "+v"(empty4));
    auto mark = [&](int i) { __builtin_amdgcn_raw_buffer_store_b128(empty4, rs, svo + (i & 1) * 128, (i >> 1) * 65536, 16); asm volatile("" ::: "memory"); };
    issue(15); issue(16);
    waitvm<8>();
    STA;
    bar();
    STA;
    issue(17); issue(18); mark(0); mark(1);
    waitvm<10>();
    STA;
    bar();
    STA;
    issue(19); issue(20); mark(2); mark(3);
    u64 tag = 0;
    auto lo = [] { sfence(); __builtin_amdgcn_s_setprio(0); sfence(); };
    auto hi = [] { sfence(); __builtin_amdgcn_s_setprio(3); sfence(); };
    f32x4 d[8];
    auto convmm = [&](auto HH) AI {
      CI(H, HH);
      constexpr int NW = 9;
      bf16x8 W[NW];
      LAS char* c1 = opaque(smem + HT_OFF + hw * HREG + ((16 * (1 - H) + l15 + q) & 31) * 16);
      LAS char* c2 = opaque(smem + HT_OFF + hw * HREG + (q & 1) * 8 * HS + ((16 * (1 - H) + l15 + 4) & 31) * 16);
      auto rd = [&](auto K) { CI(k, K); constexpr int pr = k / 3, e = k % 3; W[k % NW] = ldsA((e < 2 ? c1 + (pr + 8 * e) * HS : c2 + pr * HS)); sfence(); };
      sfor<0, NW>(rd);
      sfor<0, 24>([&](auto K) {
        CI(k, K);
        constexpr int pr = k / 3, e = k % 3;
        d[pr] = __builtin_amdgcn_mfma_f32_16x16x32_bf16(W[k % NW], e < 2 ? T1[(pr + 8 * e) & 15] : T2[pr], e == 0 ? (f32x4){0.f, 0.f, 0.f, 0.f} : d[pr], 0, 0, 0);
        sfence();
        if constexpr (k + NW < 24) rd(IC<k + NW>{});
      });
    };
    auto silu = [&](auto HH, auto R) AI {
      {
        CI(H, HH); CI(r, R);
        float t[8];
        sfor<0, 8>([&](auto I) { CI(i, I); t[i] = d[i][r] * -1.4426950408889634f; sfence(); });
        sfor<0, 8>([&](auto I) { CI(i, I); t[i] = vexp2(t[i]); });
        sfor<0, 8>([&](auto I) { CI(i, I); t[i] = vadd1(t[i]); });
        sfor<0, 8>([&](auto I) { CI(i, I); t[i] = vrcp(t[i]); });
        sfor<0, 8>([&](auto I) { CI(i, I); t[i] = vmul(t[i], d[i][r]); });
        u32x4 v = {pk2(t[0], t[1]), pk2(t[2], t[3]), pk2(t[4], t[5]), pk2(t[6], t[7])};
        v = (u32x4){umin_(v[0], EMPTY - 1), umin_(v[1], EMPTY - 1), umin_(v[2], EMPTY - 1), umin_(v[3], EMPTY - 1)};
        __builtin_amdgcn_raw_buffer_store_b128(v, rs, svo + (2 * H + (r >> 1)) * 65536 + (r & 1) * 128, 0, 16);
        asm volatile("" ::: "memory");
        sfence();
      }
    };
    const Base2 swb(smem + hw * 1024 + lane * 16);
    LAS char* swh = opaque(smem + HT_OFF + hw * HREG + lane * 16);
    auto swrite1 = [&](auto PP, auto I) {
      CI(P, PP); CI(i, I);
      if constexpr (P == 2) *(LAS u32x4*)(swh + i * 1024) = sf[0][i];
      else *(LAS u32x4*)(swb.at<sbuf(P, i >> 2) + (i & 3) * 4096>()) = sf[P & 1][i];
    };
    const bool hin = (g & 15) != 0, hout = (g & 15) != 15;
    const auto rhalo = __builtin_amdgcn_make_buffer_rsrc((void*)(ws + WS_HALO + (size_t)(g * 8 + j) * 4096), 0, hout ? 9 * 4096 : 4096, 0x00020000);
    LAS char* hh = smem + HT_OFF + hw * HREG + (lane >> 2) * HS + (lane & 3) * 16;
    waitvm<12>();
    STA;
    bar();
    STA;
    issue(21); issue(22); mark(4); mark(5);
    __builtin_amdgcn_raw_buffer_store_b128(*(LAS u32x4*)hh, rhalo, 8 * 4096 + hw * 1024 + lane * 16, 0, 16);
    waitvm<13>();
    STA;
    bar();
    STA;
    mark(6); mark(7); issue(23); issue(24);
    const int fl8 = lane < 8 || (lane == 8 && hin) || lane == 9 ? lane : 0;
    tag = *(LAS u64*)(smem + MISC_OFF + 64 + (fl8 < 8 ? 0 : fl8 - 7) * 8);
    GAS u64* fp = fl8 < 8 ? flags + fl8 * FST : (GAS u64*)(ws + WS_HFLAG + (g * 8 + j + (fl8 - 8) * 8) * 128);
    waitvm<8>();
    STA;
    bar();
    STA;
    if (hw == 0 && (lane == j || (lane == 9 && hout))) __hip_atomic_store(fp, tag, RLX_AGENT);
    issue(25); issue(26);
    waitvm<12>();
    STA;
    bar();
    STA;
    issue(27); issue(28);
    u64 fv = __hip_atomic_load(fp, RLX_AGENT);
    if (!__all(fv == tag || lane == 9)) {
      unsigned spins = 0;
#pragma unroll 1
      do { __builtin_amdgcn_s_sleep(PSL); fv = __hip_atomic_load(fp, RLX_AGENT); } while (!__all(fv == tag || lane == 9) && ++spins < (1u << 20));
    }
    asm volatile("" ::: "memory");
    const u32x4 halo = __builtin_amdgcn_raw_buffer_load_b128(rhalo, hin ? hw * 1024 + lane * 16 : -1, 0, 16);
    asm volatile("" ::: "memory");
    hi();
    STA;
    bar();
    STA;
    convmm(IC<0>{});
    silu(IC<0>{}, IC<0>{});
    *(LAS u32x4*)hh = halo;
    silu(IC<0>{}, IC<1>{});
    if (lane == 0) lds_inc(misc + 1 * 4);
    STA;
    silu(IC<0>{}, IC<2>{}); silu(IC<0>{}, IC<3>{});
    STA;
    convmm(IC<1>{});
    sfor<0, 4>([&](auto R) { silu(IC<1>{}, R); });
    HLO
    STA;
    __builtin_amdgcn_s_sleep(SLPN);
    sload(IC<2>{});
    scheck(IC<2>{});
    STA;
    sfor<0, 8>([&](auto I) { swrite1(IC<2>{}, I); sload1(IC<3>{}, I); sfence(); });
    signal(6);
    STA;
    if (hw == 0 && ((lane == 0 && j == 0) || (lane == 8 && hin)))
      __hip_atomic_store(lane == 0 ? epoch : (GAS u64*)(ws + WS_HEPOCH + (g * 8 + j) * 128), tag, RLX_AGENT);
    scheck(IC<3>{});
    await(9);
    STA;
    sfor<0, 8>([&](auto I) { swrite1(IC<3>{}, I); });
    signal(7);
    STA;
    STDUMP();
  }
}

extern "C" void kernel_launch(void* const* d_in, const int* in_sizes, int n_in, void* d_out, int out_size, void* d_ws,
                              size_t ws_size, hipStream_t stream) {
  (void)hipFuncSetAttribute((const void*)conv_module_kernel, hipFuncAttributeMaxDynamicSharedMemorySize, LDS_BYTES);
  conv_module_kernel<<<256, 512, LDS_BYTES, stream>>>((const char*)d_in[0], (const char*)d_in[1], (const char*)d_in[2],
                                                      (const char*)d_in[3], (char*)d_out, (char*)d_ws);
}
```

```cpp
#include <hip/hip_runtime.h>
#include <hip/hip_bf16.h>

typedef short s16x4 __attribute__((ext_vector_type(4)));
typedef __bf16 bf16x8 __attribute__((ext_vector_type(8)));
typedef __bf16 bf16x2 __attribute__((ext_vector_type(2)));
typedef float f32x4 __attribute__((ext_vector_type(4)));
typedef float f32x2 __attribute__((ext_vector_type(2)));
typedef unsigned u32x4 __attribute__((ext_vector_type(4)));
typedef unsigned u32x2 __attribute__((ext_vector_type(2)));
typedef unsigned long long u64;
#define LAS __attribute__((address_space(3)))
#define GAS __attribute__((address_space(1)))
#define DEVI __device__ __forceinline__
#define AI __attribute__((always_inline))
#define RLX_AGENT __ATOMIC_RELAXED, __HIP_MEMORY_SCOPE_AGENT

constexpr int T_ = 4000, TILE = 256;
constexpr int SLOT = 16896;
constexpr int NS = 7;
constexpr int HT_OFF = NS * SLOT;
constexpr int HS = 528;
constexpr int HREG = 16 * HS;
constexpr int TAB_OFF = HT_OFF + 2 * SLOT;
constexpr int TS = 176;
constexpr int MISC_OFF = TAB_OFF + 64 * TS;
constexpr int LDS_BYTES = 160 * 1024;
static_assert(MISC_OFF + 512 <= LDS_BYTES && HT_OFF + 64 * HS <= TAB_OFF);
constexpr size_t WS_FLAG = 8u << 20;
constexpr size_t WS_EPOCH = WS_FLAG + 32 * 4 * 8 * 128;
constexpr size_t WS_HEPOCH = WS_EPOCH + 32 * 128;
constexpr size_t WS_HFLAG = WS_HEPOCH + 264 * 128;
constexpr size_t WS_HALO = WS_HFLAG + 264 * 128;
constexpr size_t WS_STAMP = WS_HALO + 264 * 4096;
constexpr unsigned CODE_LINES = 217;
constexpr unsigned EMPTY = 0xFFFFFFFFu;
constexpr int PF = 3;

#define KROT gi
#define PSL 2
#define FST 16
#define S0T 1
#define HLO lo();
#define BROT 1
#define SLP
#define ORD 0
#define MARKS
#define SLPN 2
#define TSPLIT 3
#define OAUX 2
#define PUBLATE 0
#define CSLP
#define SLP3 4
#define B11S 10
#define CST
template <int N> DEVI void waitvm() { asm volatile("s_waitcnt vmcnt(%0)" ::"n"(N) : "memory"); }
template <int N> DEVI void waitlgkm() { asm volatile("s_waitcnt lgkmcnt(%0)" ::"n"(N) : "memory"); }
DEVI void bar() { asm volatile("" ::: "memory"); __builtin_amdgcn_s_barrier(); asm volatile("" ::: "memory"); }
DEVI void sfence() { __builtin_amdgcn_sched_barrier(0); }
DEVI unsigned pk2(float lo, float hi) { f32x2 p = {lo, hi}; return __builtin_bit_cast(unsigned, __builtin_convertvector(p, bf16x2)); }
DEVI bf16x8 ldsA(LAS char* p) { return *(LAS bf16x8*)p; }
DEVI s16x4 ld64(LAS char* p) { return *(LAS s16x4*)p; }
DEVI s16x4 ldtr(LAS char* p) { return __builtin_amdgcn_ds_read_tr16_b64_v4i16((LAS s16x4*)p); }
DEVI bf16x8 cat8(s16x4 a, s16x4 b) { return __builtin_bit_cast(bf16x8, __builtin_shufflevector(a, b, 0, 1, 2, 3, 4, 5, 6, 7)); }
DEVI void mf(f32x4& c, const bf16x8& a, const bf16x8& b) { asm volatile("v_mfma_f32_16x16x32_bf16 %0, %1, %2, %0" : "+v"(c) : "v"(a), "v"(b)); }
DEVI void mf0(f32x4& c, const bf16x8& a, const bf16x8& b) { asm volatile("v_mfma_f32_16x16x32_bf16 %0, %1, %2, 0" : "=&v"(c) : "v"(a), "v"(b)); }
DEVI float vnl2e(float a) { float r; asm volatile("v_mul_f32 %0, 0xbfb8aa3b, %1" : "=v"(r) : "v"(a)); return r; }
DEVI float vadd1(float a) { float r; asm volatile("v_add_f32 %0, 1.0, %1" : "=v"(r) : "v"(a)); return r; }
DEVI float vmul(float a, float b) { float r; asm volatile("v_mul_f32 %0, %1, %2" : "=v"(r) : "v"(a), "v"(b)); return r; }
DEVI float vexp2(float a) { float r; asm volatile("v_exp_f32 %0, %1" : "=v"(r) : "v"(a)); return r; }
DEVI float vrcp(float a) { float r; asm volatile("v_rcp_f32 %0, %1" : "=v"(r) : "v"(a)); return r; }
DEVI unsigned vpk(float a, float b) { unsigned r; asm volatile("v_cvt_pk_bf16_f32 %0, %1, %2" : "=v"(r) : "v"(a), "v"(b)); return r; }
DEVI LAS char* opaque(LAS char* p) { asm volatile("" : "+v"(p)); return p; }
DEVI void lds_inc(unsigned addr) { asm volatile("ds_add_u32 %0, %1" ::"v"(addr), "v"(1u) : "memory"); }
DEVI unsigned lds_inc_rtn(unsigned addr) { unsigned r; asm volatile("ds_add_rtn_u32 %0, %1, %2\n\ts_waitcnt lgkmcnt(0)" : "=&v"(r) : "v"(addr), "v"(1u) : "memory"); return r; }
DEVI unsigned umin_(unsigned a, unsigned b) { return a < b ? a : b; }
DEVI unsigned umax_(unsigned a, unsigned b) { return a > b ? a : b; }
DEVI u64 opaque64(u64 v) { asm volatile("" : "+v"(v)); return v; }
DEVI u64 rfl64(u64 v) { return (u64)(unsigned)__builtin_amdgcn_readfirstlane((int)v) | ((u64)(unsigned)__builtin_amdgcn_readfirstlane((int)(v >> 32)) << 32); }
DEVI void dma_sc1(unsigned lds, int voff, u32x4 srd, int soff) {
  unsigned keep;
  asm volatile("s_mov_b32 %0, m0\n\ts_mov_b32 m0, %1\n\ts_nop 2\n\tbuffer_load_dwordx4 %2, %3, %4 offen sc1 lds\n\ts_mov_b32 m0, %0"
               : "=&s"(keep) : "s"(lds), "v"(voff), "s"(srd), "s"(soff) : "memory");
}
template <int N> DEVI void nops() { asm volatile("s_nop %0" ::"n"(N)); }

template <int V> struct IC { static constexpr int value = V; };
template <int B, int E, class F> DEVI void sfor(F&& f) {
  if constexpr (B < E) { f(IC<B>{}); sfor<B + 1, E>(f); }
}
#define CI(name, arg) constexpr int name = decltype(arg)::value
template <int NB> struct BaseN {
  LAS char* p[NB];
  DEVI BaseN(LAS char* b) {
    p[0] = b;
#pragma unroll
    for (int i = 1; i < NB; i++) p[i] = opaque(b + i * 61440);
  }
  template <int OFF> DEVI LAS char* at() const {
    constexpr int k = OFF / 61440 < NB ? OFF / 61440 : NB - 1;
    static_assert(OFF - k * 61440 < 65536);
    return p[k] + (OFF - k * 61440);
  }
};
typedef BaseN<2> Base2;
typedef BaseN<3> Base3;

#define ST(i) do {} while (0)
#define STA do {} while (0)
#define STR(k) do {} while (0)
#define STDUMP() do {} while (0)

constexpr int wunit(int m) { return m < 4 ? m : m + 2; }
constexpr int slot_of(int n) { return (n < 7 ? n : n < 10 ? n - 7 : n < 12 ? 18 - n : n < 29 ? (n - 2) % NS : n == 29 ? 24 - 2 - 3 * NS : (n - 5) % NS) * SLOT; }
constexpr int sbuf(int P, int h) { return slot_of(P == 0 ? 22 + h : P == 1 ? 25 + h : h ? 22 : 24); }
constexpr int sfrag(int P, int hf, int ks) {
  return P == 2 ? HT_OFF + (ks & 3) * HREG + (4 * (ks >> 3) + 2 * hf + ((ks >> 2) & 1)) * 1024 : sbuf(P, ks >> 3) + (hf * 8 + (ks & 7)) * 1024;
}
constexpr int PFS_OFF = MISC_OFF + 256;

__global__ __launch_bounds__(512, 2) void conv_module_kernel(const char* __restrict__ x, const char* __restrict__ w1,
                                                             const char* __restrict__ dw, const char* __restrict__ w2,
                                                             char* out, char* ws) {
  extern __shared__ __attribute__((aligned(16))) char smem_[];
  LAS char* smem = (LAS char*)smem_;
  u64 pc0;
  asm volatile("s_getpc_b64 %0" : "=s"(pc0));
  const int bid = blockIdx.x;
  const int gi = bid >> 6, j = (bid >> 3) & 7, g = (bid & 7) * 4 + gi;
  const int wave = __builtin_amdgcn_readfirstlane(threadIdx.x >> 6);
  const int lane = threadIdx.x & 63;
  const int batch = g >> 4, t0 = (g & 15) * TILE;
  const int cg = j & 3, rh = j >> 2;
  const int q = lane >> 4, l15 = lane & 15;
  const unsigned misc = (unsigned)(size_t)(smem + MISC_OFF);
  GAS u64* epoch = (GAS u64*)(ws + WS_EPOCH + g * 128);
  ST(0);
  {
    const auto rc = __builtin_amdgcn_make_buffer_rsrc((void*)pc0, 0, CODE_LINES * 128, 0x00020000);
    __builtin_amdgcn_raw_ptr_buffer_load_lds(rc, smem + PFS_OFF, 4, 0, umin_((bid >> 3) * 8 + wave, CODE_LINES - 1) * 128, 0, 0);
  }
  const int w3 = wave & 3, i0 = (wave >> 2) * 2;
  const int lc = (l15 >> 1) ^ q ^ ((w3 >> 1) << 2);
  int vw = (4 * w3 + q) * 2048 + (lc >> 2) * 1024 + j * 128 + (lc & 3) * 32 + (lane & 1) * 16;
  asm volatile("" : "+v"(vw));
  const auto rdw = __builtin_amdgcn_make_buffer_rsrc((void*)dw, 0, 31 * 1024, 0x00020000);
  if (wave < 4) __builtin_amdgcn_raw_ptr_buffer_load_lds(rdw, smem + TAB_OFF + w3 * 16 * TS, 16, (lane >> 1) * 1024 + j * 128 + w3 * 32 + (lane & 1) * 16, 0, 0, 0);
  asm volatile("" ::: "memory");
  const char* w1p = w1;
  asm volatile("" : "+s"(w1p));
  const auto rw1 = __builtin_amdgcn_make_buffer_rsrc((void*)w1p, 0, 1024 * 1024, 0x00020000);
#pragma unroll
  for (int i = 0; i < 2; i++)
    __builtin_amdgcn_raw_ptr_buffer_load_lds(rw1, smem + slot_of(0) + w3 * 1024 + (i0 + i) * 4096, 16, vw, ((KROT & 3) * 64 + 16 * (i0 + i)) * 2048, 0, 0);
  asm volatile("" ::: "memory");
  sfence();
  const u64 ordp = (j & 4) ? ((j & 2) ? 0x1324765832146587ull : 0x2134578613247685ull) : ((j & 2) ? 0x3214765821346587ull : 0x1324578632147685ull);
  const unsigned ord = (unsigned)(ordp >> (32 * (j & 1)));
  const int vrot = 2 * gi + rh;
  auto blk = [&](int b) { return (int)((ord >> (4 * b)) & 15); };
  const auto rx = __builtin_amdgcn_make_buffer_rsrc((void*)(x + (size_t)batch * (T_ * 1024)), 0, T_ * 1024, 0x00020000);
  const int vx = (t0 + 8 * (w3 >> 1) + 2 * (w3 & 1) + (lane >> 5)) * 1024 + (((((lane & 31) ^ (2 * (w3 & 1) + (lane >> 5))) * 16) + KROT * 128) & 511);
#pragma unroll
  for (int i = 0; i < 2; i++)
      __builtin_amdgcn_raw_ptr_buffer_load_lds(rx, smem + slot_of(4) + w3 * 1024 + (w3 >> 1) * 64 + (i0 + i) * (4096 + 128), 16, vx, (blk(0) - 1) * 32768 + (16 * i + 2 * i0) * 1024, 0, 0);
  asm volatile("" ::: "memory");
  ST(18);
  const auto rs = __builtin_amdgcn_make_buffer_rsrc((void*)(ws + (size_t)g * 262144), 0, 262144, 0x00020000);
  GAS u64* flags = (GAS u64*)(ws + WS_FLAG + (size_t)g * 4096);
  const int svo = (q * 16 + 2 * j + ((wave >> 1) & 1)) * 1024 + ((2 * (wave & 1) + (l15 >> 3)) * 16 + (lane & 7)) * 16;

  asm volatile("" ::: "memory");
  const auto rw2 = __builtin_amdgcn_make_buffer_rsrc((void*)w2, 0, 512 * 1024, 0x00020000);
  const int vv = (4 * w3 + q) * 1024 + cg * 256 + (((l15 >> 2) ^ q) * 64) + (lane & 3) * 16;
  auto issueV = [&](int n, int i) {
    __builtin_amdgcn_raw_ptr_buffer_load_lds(rw2, smem + slot_of(n) + w3 * 1024 + i * 4096, 16, vv, (((n - 24 + vrot) & 7) * 64 + 16 * i) * 1024, 0, 0);
  };
  const u64 sptr = (u64)(ws + (size_t)g * 262144);
  const u32x4 srd = {(unsigned)sptr, (unsigned)(sptr >> 32) & 0xffffu, 262144u, 0x00020000u};
  const unsigned lds0 = (unsigned)(size_t)smem;
  auto sdst = [&](auto PP, auto I) AI { CI(P, PP); CI(i, I); return P == 2 ? HT_OFF + w3 * HREG + i * 1024 : sbuf(P, i >> 2) + (i & 3) * 4096 + w3 * 1024; };
  auto sload = [&](auto PP) AI {
    CI(P, PP);
    sfor<0, 8>([&](auto I) AI {
      CI(i, I);
      dma_sc1(lds0 + sdst(PP, I), lane * 16, srd, ((4 * P + 2 * rh + ((i & 3) >> 1)) * 16 + ((8 * (i >> 2) + 4 * (i & 1) + w3 + 2 * vrot) & 15)) * 1024);
    });
  };
  auto sempty = [&](auto PP) AI {
    unsigned m = 0;
    sfor<0, 8>([&](auto I) AI {
      const u32x4 v = *(LAS u32x4*)(smem + sdst(PP, I) + lane * 16);
      m = umax_(umax_(m, v[0]), v[1]); m = umax_(umax_(m, v[2]), v[3]);
    });
    return __any(m == EMPTY);
  };
  auto scheck = [&](auto PP) AI {
    unsigned spins = 0;
#pragma unroll 1
    while (sempty(PP) && ++spins < (1u << 16)) { sload(PP); waitvm<0>(); }
    asm volatile("" ::: "memory");
  };
  auto signal = [&](int w) {
    waitlgkm<0>();
    if (lane == 0) lds_inc(misc + w * 4);
  };
  auto await = [&](int w, unsigned target = 4) {
    volatile LAS unsigned* words = (volatile LAS unsigned*)(smem + MISC_OFF);
#pragma unroll 1
    for (unsigned spins = 0; spins < (1u << 24); spins++) {
      if ((unsigned)__builtin_amdgcn_readfirstlane(words[w]) == target) break;
      __builtin_amdgcn_s_sleep(1);
    }
    asm volatile("" ::: "memory");
  };

  if (wave < 4) {
    const int ng = wave;
    __builtin_amdgcn_s_setprio(2);
    const auto rout = __builtin_amdgcn_make_buffer_rsrc((void*)(out + (size_t)batch * (T_ * 1024)), 0, T_ * 1024, 0x00020000);
    waitvm<4>();
    {
      LAS char* tb = smem + TAB_OFF + ng * 16 * TS;
      unsigned tap[13];
#pragma unroll
      for (int m = 0; m < 13; m++) {
        const unsigned o = 12 * q - 9 + m;
        tap[m] = *(LAS unsigned short*)(tb + (o < 31u ? o : 31u) * 32 + l15 * 2);
      }
      waitlgkm<0>();
      LAS unsigned* tw = (LAS unsigned*)(tb + 4 + l15 * TS) + 6 * q;
#pragma unroll
      for (int m = 0; m < 12; m++) tw[(m & 1) ? 19 + (m >> 1) : (m >> 1) - 1] = tap[m] | (tap[m + 1] << 16);
      asm volatile("" ::: "memory");
      if (threadIdx.x < 16) *(LAS unsigned*)(smem + MISC_OFF + threadIdx.x * 4) = 0u;
    }
    __builtin_amdgcn_s_waitcnt(0x0F70);
    const u64 ep0 = __hip_atomic_load(lane == 0 ? epoch : (GAS u64*)(ws + WS_HEPOCH + (g * 8 + j + (lane == 2 ? 8 : 0)) * 128), RLX_AGENT);
    const Base3 abase(smem + (l15 >> 1) * 1024 + (l15 >> 2) * 64 + (l15 & 1) * 512 + ((q ^ (l15 & 3)) * 16));
    constexpr int RH = 8 * 1024 + 4 * 64;
    const int q4 = l15 >> 2, p4 = lane & 3;
    const Base2 wa(smem + (8 * q + q4) * 256 + ((ng ^ q4 ^ ((q & 1) << 2)) * 32) + p4 * 8);
    const Base2 wg(smem + (8 * q + q4) * 256 + (((4 + ng) ^ q4 ^ ((q & 1) << 2)) * 32) + p4 * 8);
    LAS char* hbase = opaque(smem + HT_OFF + (ng * 16 + l15) * HS + q * 16);
    bf16x8 Ba[16], Bg[16];
    bf16x8 A[PF][2];
    f32x4 ca[3][2], cgt[3][2];
    float e[8];
    u32x4 hp, hp0;

    auto loadW = [&](auto R) {
      CI(r, R);
      constexpr int ks = r >> 1, off = slot_of(wunit(ks >> 1)) + (ks & 1) * 8192;
      if constexpr (r & 1) Bg[ks] = cat8(ldtr(wg.at<off>()), ldtr(wg.at<off + 1024>()));
      else Ba[ks] = cat8(ldtr(wa.at<off>()), ldtr(wa.at<off + 1024>()));
    };
    auto glu = [&](auto U, auto PP, auto BP) {
      CI(u0, U); CI(pp, PP); CI(bp, BP);
      constexpr int u = u0 == 0 ? 0 : u0 == 1 ? -1 : u0 - 1;
      if constexpr (u >= 0 && u < 4) { e[2 * u] = vnl2e(cgt[pp][u >> 1][(2 * u) & 3]); e[2 * u + 1] = vnl2e(cgt[pp][u >> 1][(2 * u + 1) & 3]); }
      if constexpr (u >= 4 && u < 12) e[u - 4] = vexp2(e[u - 4]);
      if constexpr (u >= 12 && u < 16) { e[2 * u - 24] = vadd1(e[2 * u - 24]); e[2 * u - 23] = vadd1(e[2 * u - 23]); }
      if constexpr (u >= 16 && u < 24) e[u - 16] = vrcp(e[u - 16]);
      if constexpr (u >= 24 && u < 28) {
        constexpr int i = 2 * u - 48;
        e[i] = vmul(e[i], ca[pp][i >> 2][i & 3]); e[i + 1] = vmul(e[i + 1], ca[pp][i >> 2][(i & 3) + 1]);
      }
      if constexpr (u == 28 || u == 29) { constexpr int i = 2 * u - 56; hp[i] = vpk(e[2 * i], e[2 * i + 1]); hp[i + 1] = vpk(e[2 * i + 2], e[2 * i + 3]); }
      if constexpr (u == 30) { if constexpr (bp == 0) hp0 = hp; else *(LAS u32x4*)(hbase + (blk(bp) & 7) * 64) = hp; }
    };
    auto step = [&](auto I, auto PP, auto KS, auto FIRST, auto NOFF, auto&& f0, auto&& f1) {
      CI(i, I); CI(pp, PP); CI(ks, KS); CI(first, FIRST); CI(noff, NOFF);
      if constexpr (first) mf0(ca[pp][0], A[i][0], Ba[ks]); else mf(ca[pp][0], A[i][0], Ba[ks]);
      sfence(); f0(); sfence();
      if constexpr (first) mf0(cgt[pp][0], A[i][0], Bg[ks]); else mf(cgt[pp][0], A[i][0], Bg[ks]);
      sfence();
      if constexpr (noff >= 0) A[i][0] = ldsA(abase.at<noff>());
      sfence();
      if constexpr (first) mf0(cgt[pp][1], A[i][1], Bg[ks]); else mf(cgt[pp][1], A[i][1], Bg[ks]);
      sfence(); f1(); sfence();
      if constexpr (first) mf0(ca[pp][1], A[i][1], Ba[ks]); else mf(ca[pp][1], A[i][1], Ba[ks]);
      sfence();
      if constexpr (noff >= 0) A[i][1] = ldsA(abase.at<noff + RH>());
      sfence();
    };
    auto nop = [] {};
    auto primeA = [&](auto N) {
      CI(n, N);
      sfor<0, PF>([&](auto I) { CI(i, I); A[i][0] = ldsA(abase.at<slot_of(n) + i * 64>()); A[i][1] = ldsA(abase.at<slot_of(n) + i * 64 + RH>()); });
    };

    waitlgkm<0>();
    ST(4);
    sfor<0, 2>([&](auto HH) {
      CI(hf, HH);
      auto aoff = [](int s) { return s < 16 ? slot_of(4 + 6 * hf + ((s >> 1) & 1)) + (2 * (s >> 2) + (s & 1)) * 64 : hf ? slot_of(12) + (s - 16) * 64 : -1; };
      sfor<0, 16>([&](auto S) {
        CI(s, S);
        if constexpr (s == 0) {
          bar();
          ST(1 + 2 * hf);
          sfor<16 * hf, 16 * hf + 4>(loadW);
          sfor<0, PF>([&](auto I) { CI(i, I); A[i][0] = ldsA(abase.at<aoff(i)>()); A[i][1] = ldsA(abase.at<aoff(i) + RH>()); });
        }
        if constexpr ((s & 3) == 2 && s < 12) bar();
        if constexpr (hf == 1 && s == 16 - PF) bar();
        step(IC<s % PF>{}, IC<(s >> 1) & 1>{}, IC<8 * hf + 2 * (s >> 2) + (s & 1)>{}, IC<hf == 0 && (s == 0 || s == 2)>{}, IC<aoff(s + PF)>{},
             [&] { if constexpr ((s & 2) && s < 12) loadW(IC<16 * hf + (s & ~3) + 4 + 2 * (s & 1)>{}); },
             [&] { if constexpr ((s & 2) && s < 12) loadW(IC<16 * hf + (s & ~3) + 5 + 2 * (s & 1)>{}); });
      });
      if constexpr (hf == 0) ST(2);
    });
    ST(5); STR(0);
    const Base2 vb(smem + (8 * q + q4) * 256 + ((ng ^ q4) * 64) + p4 * 16);
    auto loadV = [&](auto R) {
      CI(r, R);
      constexpr int ks = r >> 1, off = slot_of(24 + (ks >> 1)) + (ks & 1) * 8192 + (r & 1) * 8;
      if constexpr (r & 1) Bg[ks] = cat8(ldtr(vb.at<off>()), ldtr(vb.at<off + 1024>()));
      else Ba[ks] = cat8(ldtr(vb.at<off>()), ldtr(vb.at<off + 1024>()));
    };
    sfor<2, 6>([&](auto BB) {
      CI(b, BB);
      sfor<0, 16>([&](auto S) {
        CI(s, S);
        constexpr int n = s + PF, gs = 16 * (b - 1) + s;
        if constexpr (s == 3) { waitlgkm<6>(); bar(); }
        CST
        auto fill = [&](auto G) {
          CI(gp, G);
          if constexpr (b == 2) {
            if constexpr (gp >= 1 && gp < 17) { glu(IC<2 * gp - 2>{}, IC<0>{}, IC<0>{}); glu(IC<2 * gp - 1>{}, IC<0>{}, IC<0>{}); }
            if constexpr (gp == 16) glu(IC<0>{}, IC<1>{}, IC<1>{});
            if constexpr (gp >= 17) { glu(IC<2 * gp - 33>{}, IC<1>{}, IC<1>{}); glu(IC<2 * gp - 32>{}, IC<1>{}, IC<1>{}); }
          } else {
            glu(IC<gp + 31>{}, IC<(b + 1) % 3>{}, IC<b - 2>{});
            glu(IC<gp - 1>{}, IC<(b + 2) % 3>{}, IC<b - 1>{});
          }
          if constexpr (b == 3 && gp == 2) *(LAS u32x4*)(hbase + (blk(0) & 7) * 64) = hp0;
          if constexpr (b == 2 && gp == 0) {
            const u64 tg = opaque64(ep0) + 0x9E3779B97F4A7C15ull;
            if (threadIdx.x < 3) *(LAS u64*)(smem + MISC_OFF + 64 + threadIdx.x * 8) = tg;
          }
        };
        step(IC<gs % PF>{}, IC<b % 3>{}, IC<s>{}, IC<s == 0>{}, IC<slot_of(8 + 2 * b + (n >> 3)) + (n & 7) * 64>{},
             [&] { fill(IC<2 * s>{}); }, [&] { fill(IC<2 * s + 1>{}); });
      });
    });
    sfor<0, 32>([&](auto S) {
      CI(s, S);
      constexpr int n = s + PF, gs = 80 + s, set = (s >> 3) & 1, ks = (s >> 4) * 8 + (s & 7);
      if constexpr (s == 3) waitlgkm<6>();
      if constexpr (s == 3 || s == 19) bar();
      auto fill = [&](auto G) {
        CI(gp, G);
        glu(IC<gp + 31>{}, IC<1>{}, IC<4>{});
        glu(IC<gp - 1>{}, IC<2>{}, IC<5>{});
        if constexpr (gp >= 38 && gp < 50) loadV(IC<gp - 38>{});
        if constexpr (gp >= 50) glu(IC<gp - 50>{}, IC<0>{}, IC<6>{});
      };
      step(IC<gs % PF>{}, IC<set>{}, IC<ks>{}, IC<ks == 0>{}, IC<(n < 32 ? slot_of(20 + (n >> 3)) + (n & 7) * 64 : -1)>{},
           [&] { fill(IC<2 * s>{}); }, [&] { fill(IC<2 * s + 1>{}); });
    });
    ST(6);
    sfor<14, 32>([&](auto U) { glu(U, IC<0>{}, IC<6>{}); });
    sfor<0, 32>([&](auto U) { glu(U, IC<1>{}, IC<7>{}); });
    waitlgkm<0>();
    ST(17);
    bar();
    ST(7); STR(1);
    const Base3 sbase(smem + lane * 16);
    const int ovo = (t0 - 15 + rh * 64 + l15) * 1024 + cg * 256 + ng * 64 + q * 16;
    unsigned op[4];
    auto ostep = [&](auto U, auto PP, auto PC) {
      CI(u, U); CI(pp, PP); CI(pc, PC);
      constexpr int hf = u / 3, s = u % 3;
      if constexpr (u < 6) {
        if constexpr (s == 0) { op[0] = vpk(ca[pp][hf][0], ca[pp][hf][1]); op[1] = vpk(ca[pp][hf][2], ca[pp][hf][3]); }
        if constexpr (s == 1) { op[2] = vpk(cgt[pp][hf][0], cgt[pp][hf][1]); op[3] = vpk(cgt[pp][hf][2], cgt[pp][hf][3]); }
        if constexpr (s == 2) __builtin_amdgcn_raw_buffer_store_b128((u32x4){op[0], op[1], op[2], op[3]}, rout, ovo + ((1 - (pc >> 1)) * 128 + hf * 32 + (pc & 1) * 16) * 1024, 0, OAUX);
      }
    };
    CSLP
    sload(IC<0>{});
    sfor<12, 20>(loadV);
    sfence();
    sfor<0, 4>([&](auto I) { CI(i, I); issueV(29, i); });
    sload(IC<1>{});
    waitvm<12>();
    ST(14);
    scheck(IC<0>{});
    __builtin_amdgcn_s_waitcnt(0x0070);
    waitvm<0>();
    scheck(IC<1>{});
    ST(15);
    bar();
    ST(8);
    sfor<0, PF>([&](auto I) { CI(i, I); A[i][0] = ldsA(sbase.at<sfrag(0, 0, i)>()); A[i][1] = ldsA(sbase.at<sfrag(0, 1, i)>()); });
    sfor<0, 64>([&](auto S) {
      CI(gs, S);
      constexpr int P = gs >> 4, s = gs & 15, i = gs % PF, pp = P & 1, n = gs + PF;
      if constexpr (gs == B11S || n == 32 || n == 48) {
        bar();
        ST(9 + (gs >> 4));
      }
      if constexpr (s == 0) mf0(ca[pp][0], Ba[s], A[i][0]); else mf(ca[pp][0], Ba[s], A[i][0]);
      sfence();
      if constexpr (P > 0 && s >= 2) ostep(IC<s - 2>{}, IC<pp ^ 1>{}, IC<P - 1>{});
      if constexpr (gs < 2) loadV(IC<20 + 2 * gs>{});
      if constexpr (gs >= B11S && gs < B11S + 4) loadV(IC<24 + 2 * (gs - B11S)>{});
      sfence();
      if constexpr (s == 0) mf0(cgt[pp][0], Bg[s], A[i][0]); else mf(cgt[pp][0], Bg[s], A[i][0]);
      sfence();
      if constexpr (n < 64) A[i][0] = ldsA(sbase.at<sfrag((n & 63) >> 4, 0, n & 15)>());
      sfence();
      if constexpr (s == 0) mf0(cgt[pp][1], Bg[s], A[i][1]); else mf(cgt[pp][1], Bg[s], A[i][1]);
      sfence();
      if constexpr (gs < 2) loadV(IC<21 + 2 * gs>{});
      if constexpr (gs >= B11S && gs < B11S + 4) loadV(IC<25 + 2 * (gs - B11S)>{});
      sfence();
      if constexpr (s == 0) mf0(ca[pp][1], Ba[s], A[i][1]); else mf(ca[pp][1], Ba[s], A[i][1]);
      sfence();
      if constexpr (n < 64) A[i][1] = ldsA(sbase.at<sfrag((n & 63) >> 4, 1, n & 15)>());
      sfence();
    });
    ST(12);
    nops<7>();
    sfor<0, 6>([&](auto U) { ostep(U, IC<1>{}, IC<3>{}); nops<1>(); });
    ST(13);
    STDUMP();
  } else {
    const int hw = wave - 4;
    auto issue1 = [&](int n, int i) {
      if (n < 4 || (n >= 6 && n < 10)) {
        const int m = n < 4 ? n : n - 2;
        __builtin_amdgcn_raw_ptr_buffer_load_lds(rw1, smem + slot_of(n) + hw * 1024 + i * 4096, 16, vw, (((m & 4) + ((m + KROT) & 3)) * 64 + 16 * i) * 2048, 0, 0);
      } else if (n < 24) {
        const int b = n == 4 || n == 10 ? 0 : n == 5 || n == 11 ? 1 : n < 20 ? (n - 8) >> 1 : 6 + (n & 1);
        const int half = n < 6 ? 0 : n < 12 ? 1 : n < 20 ? (n & 1) : (n - 20) >> 1;
        __builtin_amdgcn_raw_ptr_buffer_load_lds(rx, smem + slot_of(n) + hw * 1024 + (hw >> 1) * 64 + i * (4096 + 128), 16, vx,
                                                 (blk(b) - 1) * 32768 + half * 512 + (16 * (i & 1) + 4 * (i >> 1)) * 1024, 0, 0);
      } else {
        issueV(n, i);
      }
    };
    auto issue = [&](int n) {
#pragma unroll
      for (int i = 0; i < 4; i++) issue1(n, i);
      asm volatile("" ::: "memory");
    };
    STA;
    bf16x8 T1[16], T2[8];
    auto loadT = [&](auto G) {
      LAS char* tb = smem + TAB_OFF + hw * 16 * TS;
      const int b7 = lane & 7, h1 = l15 >> 3;
      auto idx = [](int o) { return (o & 1) ? (o + 7) >> 1 : 23 + (o >> 1); };
      CI(gg, G);
      LAS unsigned* p = (LAS unsigned*)opaque(tb + 4 + (8 * h1 + 4 * (gg & 1)) * TS + (gg < 2 ? idx(8 * q - b7 - 2) : q == h1 ? idx(30 - b7) : 19) * 4);
      sfor<0, 4>([&](auto QD) {
        CI(qd, QD);
        const u32x4 v = {p[qd * 44], p[qd * 44 + 1], p[qd * 44 + 2], p[qd * 44 + 3]};
        sfence();
        if constexpr (gg < 2) T1[4 * gg + qd] = __builtin_bit_cast(bf16x8, v); else T2[4 * (gg - 2) + qd] = __builtin_bit_cast(bf16x8, v);
      });
    };
    auto splitT = [&](auto PR) {
      CI(pr, PR);
      const unsigned mh = 0u - (unsigned)(l15 >> 3);
      u32x4 v = __builtin_bit_cast(u32x4, T1[pr]);
      u32x4 a = v & ~mh, b = v & mh;
      asm volatile("" : "+v"(a), "+v"(b));
      T1[pr] = __builtin_bit_cast(bf16x8, a); T1[pr + 8] = __builtin_bit_cast(bf16x8, b);
    };
    issue(5); issue(1); waitvm<4>(); STA; bar();
    issue(2); issue(3); waitvm<8>(); bar();
    issue(6); waitvm<8>(); bar();
    issue(10); waitvm<8>(); STA; bar();
    sfor<0, 4>(loadT);
    STA;
    issue(11); issue(7); waitvm<4>(); STA; bar();
    issue(8); issue(9); waitvm<8>(); bar();
    issue(12); waitvm<8>(); bar();
    issue(13); waitvm<8>(); STA; bar();
    issue(14); waitvm<8>(); STA; bar();
    u32x4 empty4 = {EMPTY, EMPTY, EMPTY, EMPTY};
    asm volatile("" : "+v"(empty4));
    auto mark = [&](int i) { __builtin_amdgcn_raw_buffer_store_b128(empty4, rs, svo + (i & 1) * 128, (i >> 1) * 65536, 16); asm volatile("" ::: "memory"); };
    issue(15); issue(16);
    waitvm<8>();
    STA;
    bar();
    STA;
    issue(17); issue(18); mark(0); mark(1);
    sfor<0, 4>(splitT);
    waitvm<10>();
    STA;
    bar();
    STA;
    issue(19); issue(20); mark(2); mark(3);
    sfor<4, 8>(splitT);
    u64 tag = 0;
    auto lo = [] { sfence(); __builtin_amdgcn_s_setprio(0); sfence(); };
    auto hi = [] { sfence(); __builtin_amdgcn_s_setprio(3); sfence(); };
    f32x4 d[8];
    auto convmm = [&](auto HH) AI {
      CI(H, HH);
      constexpr int NW = 9;
      bf16x8 W[NW];
      LAS char* c1 = opaque(smem + HT_OFF + hw * HREG + ((16 * (1 - H) + l15 + q) & 31) * 16);
      LAS char* c2 = opaque(smem + HT_OFF + hw * HREG + (q & 1) * 8 * HS + ((16 * (1 - H) + l15 + 4) & 31) * 16);
      auto rd = [&](auto K) { CI(k, K); constexpr int pr = k / 3, e = k % 3; W[k % NW] = ldsA((e < 2 ? c1 + (pr + 8 * e) * HS : c2 + pr * HS)); sfence(); };
      sfor<0, NW>(rd);
      sfor<0, 24>([&](auto K) {
        CI(k, K);
        constexpr int pr = k / 3, e = k % 3;
        d[pr] = __builtin_amdgcn_mfma_f32_16x16x32_bf16(W[k % NW], e < 2 ? T1[(pr + 8 * e) & 15] : T2[pr], e == 0 ? (f32x4){0.f, 0.f, 0.f, 0.f} : d[pr], 0, 0, 0);
        sfence();
        if constexpr (k + NW < 24) rd(IC<k + NW>{});
      });
    };
    auto silu = [&](auto HH, auto R) AI {
      {
        CI(H, HH); CI(r, R);
        float t[8];
        sfor<0, 8>([&](auto I) { CI(i, I); t[i] = d[i][r] * -1.4426950408889634f; sfence(); });
        sfor<0, 8>([&](auto I) { CI(i, I); t[i] = vexp2(t[i]); });
        sfor<0, 8>([&](auto I) { CI(i, I); t[i] = vadd1(t[i]); });
        sfor<0, 8>([&](auto I) { CI(i, I); t[i] = vrcp(t[i]); });
        sfor<0, 8>([&](auto I) { CI(i, I); t[i] = vmul(t[i], d[i][r]); });
        u32x4 v = {pk2(t[0], t[1]), pk2(t[2], t[3]), pk2(t[4], t[5]), pk2(t[6], t[7])};
        v = (u32x4){umin_(v[0], EMPTY - 1), umin_(v[1], EMPTY - 1), umin_(v[2], EMPTY - 1), umin_(v[3], EMPTY - 1)};
        __builtin_amdgcn_raw_buffer_store_b128(v, rs, svo + (2 * H + (r >> 1)) * 65536 + (r & 1) * 128, 0, 16);
        asm volatile("" ::: "memory");
        sfence();
      }
    };
    const bool hin = (g & 15) != 0, hout = (g & 15) != 15;
    const auto rhalo = __builtin_amdgcn_make_buffer_rsrc((void*)(ws + WS_HALO + (size_t)(g * 8 + j) * 4096), 0, hout ? 9 * 4096 : 4096, 0x00020000);
    LAS char* hh = smem + HT_OFF + hw * HREG + (lane >> 2) * HS + (lane & 3) * 16;
    waitvm<12>();
    STA;
    bar();
    STA;
    __builtin_amdgcn_raw_buffer_store_b128(*(LAS u32x4*)hh, rhalo, 8 * 4096 + hw * 1024 + lane * 16, 0, 16);
    asm volatile("" ::: "memory");
    issue(21); issue(22); mark(4); mark(5);
    const int fl8 = lane < 8 || (lane == 8 && hin) || lane == 9 ? lane : 0;
    tag = *(LAS u64*)(smem + MISC_OFF + 64 + (fl8 < 8 ? 0 : fl8 - 7) * 8);
    GAS u64* fp = fl8 < 8 ? flags + fl8 * FST : (GAS u64*)(ws + WS_HFLAG + (g * 8 + j + (fl8 - 8) * 8) * 128);
    auto flagck = [&](GAS u64* p, u64 v) AI {
      if (!__all(v == tag || lane == 9)) {
        unsigned spins = 0;
#pragma unroll 1
        do { __builtin_amdgcn_s_sleep(PSL); v = __hip_atomic_load(p, RLX_AGENT); } while (!__all(v == tag || lane == 9) && ++spins < (1u << 20));
      }
      asm volatile("" ::: "memory");
    };
    waitvm<10>();
    STA;
    bar();
    STA;
    if (hw == 0 && (lane == j || (lane == 9 && hout))) __hip_atomic_store(fp, tag, RLX_AGENT);
    issue(23); issue(24); mark(6); mark(7);
    waitvm<12>();
    STA;
    bar();
    STA;
    issue(25); issue(26);
    u64 fv = __hip_atomic_load(fp, RLX_AGENT);
    hi();
    convmm(IC<0>{});
    silu(IC<0>{}, IC<0>{});
    flagck(fp, fv);
    const u32x4 halo = __builtin_amdgcn_raw_buffer_load_b128(rhalo, hin ? hw * 1024 + lane * 16 : -1, 0, 16);
    asm volatile("" ::: "memory");
    sfor<1, 4>([&](auto R) { silu(IC<0>{}, R); });
    lo();
    waitvm<6>();
    STA;
    bar();
    STA;
    GAS u64* fp2 = fl8 < 8 ? fp + 128 : fp;
    if (hw == 0 && lane == j) __hip_atomic_store(fp2, tag, RLX_AGENT);
    *(LAS u32x4*)hh = halo;
    issue(27); issue(28);
    waitvm<0>();
    hi();
    STA;
    bar();
    STA;
    convmm(IC<1>{});
    fv = __hip_atomic_load(fp2, RLX_AGENT);
    asm volatile("" ::: "memory");
    sfor<0, 4>([&](auto R) { silu(IC<1>{}, R); });
    lo();
    flagck(fp2, fv);
    STA;
    bar();
    STA;
    issue(30); issue(31);
    waitvm<0>();
    STA;
    bar();
    __builtin_amdgcn_s_sleep(SLPN);
    sload(IC<2>{});
    __builtin_amdgcn_s_sleep(SLP3);
    sload(IC<3>{});
    waitvm<8>();
    STA;
    scheck(IC<2>{});
    STA;
    bar();
    waitvm<0>();
    scheck(IC<3>{});
    STA;
    bar();
    if (hw == 0 && ((lane == 0 && j == 0) || (lane == 8 && hin)))
      __hip_atomic_store(lane == 0 ? epoch : (GAS u64*)(ws + WS_HEPOCH + (g * 8 + j) * 128), tag, RLX_AGENT);
    STDUMP();
  }
}

extern "C" void kernel_launch(void* const* d_in, const int* in_sizes, int n_in, void* d_out, int out_size, void* d_ws,
                              size_t ws_size, hipStream_t stream) {
  (void)hipFuncSetAttribute((const void*)conv_module_kernel, hipFuncAttributeMaxDynamicSharedMemorySize, LDS_BYTES);
  conv_module_kernel<<<256, 512, LDS_BYTES, stream>>>((const char*)d_in[0], (const char*)d_in[1], (const char*)d_in[2],
                                                      (const char*)d_in[3], (char*)d_out, (char*)d_ws);
}
```

```cpp
#include <hip/hip_runtime.h>
#include <hip/hip_bf16.h>

typedef short s16x4 __attribute__((ext_vector_type(4)));
typedef __bf16 bf16x8 __attribute__((ext_vector_type(8)));
typedef __bf16 bf16x2 __attribute__((ext_vector_type(2)));
typedef float f32x4 __attribute__((ext_vector_type(4)));
typedef float f32x2 __attribute__((ext_vector_type(2)));
typedef unsigned u32x4 __attribute__((ext_vector_type(4)));
typedef unsigned u32x2 __attribute__((ext_vector_type(2)));
typedef unsigned long long u64;
#define LAS __attribute__((address_space(3)))
#define GAS __attribute__((address_space(1)))
#define DEVI __device__ __forceinline__
#define AI __attribute__((always_inline))
#define RLX_AGENT __ATOMIC_RELAXED, __HIP_MEMORY_SCOPE_AGENT

constexpr int T_ = 4000, TILE = 256;
constexpr int SLOT = 16896;
constexpr int NS = 7;
constexpr int HT_OFF = NS * SLOT;
constexpr int HS = 528;
constexpr int HREG = 16 * HS;
constexpr int TAB_OFF = HT_OFF + 2 * SLOT;
constexpr int TS = 176;
constexpr int MISC_OFF = TAB_OFF + 64 * TS;
constexpr int LDS_BYTES = 160 * 1024;
static_assert(MISC_OFF + 512 <= LDS_BYTES && HT_OFF + 64 * HS <= TAB_OFF);
constexpr size_t WS_FLAG = 8u << 20;
constexpr size_t WS_EPOCH = WS_FLAG + 32 * 4 * 8 * 128;
constexpr size_t WS_HEPOCH = WS_EPOCH + 32 * 128;
constexpr size_t WS_HFLAG = WS_HEPOCH + 264 * 128;
constexpr size_t WS_HALO = WS_HFLAG + 264 * 128;
constexpr size_t WS_STAMP = WS_HALO + 264 * 4096;
constexpr unsigned CODE_LINES = 217;
constexpr unsigned EMPTY = 0xFFFFFFFFu;
constexpr int PF = 3;

#define KROT gi
#define PSL 2
#define FST 16
#define S0T 1
#define HLO lo();
#define BROT 1
#define SLP
#define ORD 0
#define MARKS
#define SLPN 3
#define TSPLIT 3
#define OAUX 2
#define PUBLATE 0
#define CSLP
#define SLP3 4
#define B11S 10
#define CST
template <int N> DEVI void waitvm() { asm volatile("s_waitcnt vmcnt(%0)" ::"n"(N) : "memory"); }
template <int N> DEVI void waitlgkm() { asm volatile("s_waitcnt lgkmcnt(%0)" ::"n"(N) : "memory"); }
DEVI void bar() { asm volatile("" ::: "memory"); __builtin_amdgcn_s_barrier(); asm volatile("" ::: "memory"); }
DEVI void sfence() { __builtin_amdgcn_sched_barrier(0); }
DEVI unsigned pk2(float lo, float hi) { f32x2 p = {lo, hi}; return __builtin_bit_cast(unsigned, __builtin_convertvector(p, bf16x2)); }
DEVI bf16x8 ldsA(LAS char* p) { return *(LAS bf16x8*)p; }
DEVI s16x4 ld64(LAS char* p) { return *(LAS s16x4*)p; }
DEVI s16x4 ldtr(LAS char* p) { return __builtin_amdgcn_ds_read_tr16_b64_v4i16((LAS s16x4*)p); }
DEVI bf16x8 cat8(s16x4 a, s16x4 b) { return __builtin_bit_cast(bf16x8, __builtin_shufflevector(a, b, 0, 1, 2, 3, 4, 5, 6, 7)); }
DEVI void mf(f32x4& c, const bf16x8& a, const bf16x8& b) { asm volatile("v_mfma_f32_16x16x32_bf16 %0, %1, %2, %0" : "+v"(c) : "v"(a), "v"(b)); }
DEVI void mf0(f32x4& c, const bf16x8& a, const bf16x8& b) { asm volatile("v_mfma_f32_16x16x32_bf16 %0, %1, %2, 0" : "=&v"(c) : "v"(a), "v"(b)); }
DEVI float vnl2e(float a) { float r; asm volatile("v_mul_f32 %0, 0xbfb8aa3b, %1" : "=v"(r) : "v"(a)); return r; }
DEVI float vadd1(float a) { float r; asm volatile("v_add_f32 %0, 1.0, %1" : "=v"(r) : "v"(a)); return r; }
DEVI float vmul(float a, float b) { float r; asm volatile("v_mul_f32 %0, %1, %2" : "=v"(r) : "v"(a), "v"(b)); return r; }
DEVI float vexp2(float a) { float r; asm volatile("v_exp_f32 %0, %1" : "=v"(r) : "v"(a)); return r; }
DEVI float vrcp(float a) { float r; asm volatile("v_rcp_f32 %0, %1" : "=v"(r) : "v"(a)); return r; }
DEVI unsigned vpk(float a, float b) { unsigned r; asm volatile("v_cvt_pk_bf16_f32 %0, %1, %2" : "=v"(r) : "v"(a), "v"(b)); return r; }
DEVI LAS char* opaque(LAS char* p) { asm volatile("" : "+v"(p)); return p; }
DEVI void lds_inc(unsigned addr) { asm volatile("ds_add_u32 %0, %1" ::"v"(addr), "v"(1u) : "memory"); }
DEVI unsigned lds_inc_rtn(unsigned addr) { unsigned r; asm volatile("ds_add_rtn_u32 %0, %1, %2\n\ts_waitcnt lgkmcnt(0)" : "=&v"(r) : "v"(addr), "v"(1u) : "memory"); return r; }
DEVI unsigned umin_(unsigned a, unsigned b) { return a < b ? a : b; }
DEVI unsigned umax_(unsigned a, unsigned b) { return a > b ? a : b; }
DEVI u64 opaque64(u64 v) { asm volatile("" : "+v"(v)); return v; }
DEVI u64 rfl64(u64 v) { return (u64)(unsigned)__builtin_amdgcn_readfirstlane((int)v) | ((u64)(unsigned)__builtin_amdgcn_readfirstlane((int)(v >> 32)) << 32); }
DEVI void dma_sc1(unsigned lds, int voff, u32x4 srd, int soff) {
  unsigned keep;
  asm volatile("s_mov_b32 %0, m0\n\ts_mov_b32 m0, %1\n\ts_nop 2\n\tbuffer_load_dwordx4 %2, %3, %4 offen sc1 lds\n\ts_mov_b32 m0, %0"
               : "=&s"(keep) : "s"(lds), "v"(voff), "s"(srd), "s"(soff) : "memory");
}
template <int N> DEVI void nops() { asm volatile("s_nop %0" ::"n"(N)); }

template <int V> struct IC { static constexpr int value = V; };
template <int B, int E, class F> DEVI void sfor(F&& f) {
  if constexpr (B < E) { f(IC<B>{}); sfor<B + 1, E>(f); }
}
#define CI(name, arg) constexpr int name = decltype(arg)::value
template <int NB> struct BaseN {
  LAS char* p[NB];
  DEVI BaseN(LAS char* b) {
    p[0] = b;
#pragma unroll
    for (int i = 1; i < NB; i++) p[i] = opaque(b + i * 61440);
  }
  template <int OFF> DEVI LAS char* at() const {
    constexpr int k = OFF / 61440 < NB ? OFF / 61440 : NB - 1;
    static_assert(OFF - k * 61440 < 65536);
    return p[k] + (OFF - k * 61440);
  }
};
typedef BaseN<2> Base2;
typedef BaseN<3> Base3;

#define ST(i) do {} while (0)
#define STA do {} while (0)
#define STR(k) do {} while (0)
#define STDUMP() do {} while (0)

constexpr int wunit(int m) { return m < 4 ? m : m + 2; }
constexpr int slot_of(int n) { return (n < 7 ? n : n < 10 ? n - 7 : n < 12 ? 18 - n : n < 29 ? (n - 2) % NS : n == 29 ? 24 - 2 - 3 * NS : (n - 5) % NS) * SLOT; }
constexpr int sbuf(int P, int h) { return slot_of(P == 0 ? 22 + h : P == 1 ? 25 + h : h ? 22 : 24); }
constexpr int sfrag(int P, int hf, int ks) {
  return P == 2 ? HT_OFF + (ks & 3) * HREG + (4 * (ks >> 3) + 2 * hf + ((ks >> 2) & 1)) * 1024 : sbuf(P, ks >> 3) + (hf * 8 + (ks & 7)) * 1024;
}
constexpr int PFS_OFF = MISC_OFF + 256;

__global__ __launch_bounds__(512, 2) void conv_module_kernel(const char* __restrict__ x, const char* __restrict__ w1,
                                                             const char* __restrict__ dw, const char* __restrict__ w2,
                                                             char* out, char* ws) {
  extern __shared__ __attribute__((aligned(16))) char smem_[];
  LAS char* smem = (LAS char*)smem_;
  u64 pc0;
  asm volatile("s_getpc_b64 %0" : "=s"(pc0));
  const int bid = blockIdx.x;
  const int gi = bid >> 6, j = (bid >> 3) & 7, g = (bid & 7) * 4 + gi;
  const int wave = __builtin_amdgcn_readfirstlane(threadIdx.x >> 6);
  const int lane = threadIdx.x & 63;
  const int batch = g >> 4, t0 = (g & 15) * TILE;
  const int cg = j & 3, rh = j >> 2;
  const int q = lane >> 4, l15 = lane & 15;
  const unsigned misc = (unsigned)(size_t)(smem + MISC_OFF);
  GAS u64* epoch = (GAS u64*)(ws + WS_EPOCH + g * 128);
  ST(0);
  {
    const auto rc = __builtin_amdgcn_make_buffer_rsrc((void*)pc0, 0, CODE_LINES * 128, 0x00020000);
    __builtin_amdgcn_raw_ptr_buffer_load_lds(rc, smem + PFS_OFF, 4, 0, umin_((bid >> 3) * 8 + wave, CODE_LINES - 1) * 128, 0, 0);
  }
  const int w3 = wave & 3, i0 = (wave >> 2) * 2;
  const int lc = (l15 >> 1) ^ q ^ ((w3 >> 1) << 2);
  int vw = (4 * w3 + q) * 2048 + (lc >> 2) * 1024 + j * 128 + (lc & 3) * 32 + (lane & 1) * 16;
  asm volatile("" : "+v"(vw));
  const auto rdw = __builtin_amdgcn_make_buffer_rsrc((void*)dw, 0, 31 * 1024, 0x00020000);
  if (wave < 4) __builtin_amdgcn_raw_ptr_buffer_load_lds(rdw, smem + TAB_OFF + w3 * 16 * TS, 16, (lane >> 1) * 1024 + j * 128 + w3 * 32 + (lane & 1) * 16, 0, 0, 0);
  asm volatile("" ::: "memory");
  const char* w1p = w1;
  asm volatile("" : "+s"(w1p));
  const auto rw1 = __builtin_amdgcn_make_buffer_rsrc((void*)w1p, 0, 1024 * 1024, 0x00020000);
#pragma unroll
  for (int i = 0; i < 2; i++)
    __builtin_amdgcn_raw_ptr_buffer_load_lds(rw1, smem + slot_of(0) + w3 * 1024 + (i0 + i) * 4096, 16, vw, ((KROT & 3) * 64 + 16 * (i0 + i)) * 2048, 0, 0);
  asm volatile("" ::: "memory");
  sfence();
  const u64 ordp = (j & 4) ? ((j & 2) ? 0x1324765832146587ull : 0x2134578613247685ull) : ((j & 2) ? 0x3214765821346587ull : 0x1324578632147685ull);
  const unsigned ord = (unsigned)(ordp >> (32 * (j & 1)));
  const int vrot = 2 * gi + rh;
  auto blk = [&](int b) { return (int)((ord >> (4 * b)) & 15); };
  const auto rx = __builtin_amdgcn_make_buffer_rsrc((void*)(x + (size_t)batch * (T_ * 1024)), 0, T_ * 1024, 0x00020000);
  const int vx = (t0 + 8 * (w3 >> 1) + 2 * (w3 & 1) + (lane >> 5)) * 1024 + (((((lane & 31) ^ (2 * (w3 & 1) + (lane >> 5))) * 16) + KROT * 128) & 511);
#pragma unroll
  for (int i = 0; i < 2; i++)
      __builtin_amdgcn_raw_ptr_buffer_load_lds(rx, smem + slot_of(4) + w3 * 1024 + (w3 >> 1) * 64 + (i0 + i) * (4096 + 128), 16, vx, (blk(0) - 1) * 32768 + (16 * i + 2 * i0) * 1024, 0, 0);
  asm volatile("" ::: "memory");
  ST(18);
  const auto rs = __builtin_amdgcn_make_buffer_rsrc((void*)(ws + (size_t)g * 262144), 0, 262144, 0x00020000);
  GAS u64* flags = (GAS u64*)(ws + WS_FLAG + (size_t)g * 4096);
  const int svo = (q * 16 + 2 * j + ((wave >> 1) & 1)) * 1024 + ((2 * (wave & 1) + (l15 >> 3)) * 16 + (lane & 7)) * 16;

  asm volatile("" ::: "memory");
  const auto rw2 = __builtin_amdgcn_make_buffer_rsrc((void*)w2, 0, 512 * 1024, 0x00020000);
  const int vv = (4 * w3 + q) * 1024 + cg * 256 + (((l15 >> 2) ^ q) * 64) + (lane & 3) * 16;
  auto issueV = [&](int n, int i) {
    __builtin_amdgcn_raw_ptr_buffer_load_lds(rw2, smem + slot_of(n) + w3 * 1024 + i * 4096, 16, vv, (((n - 24 + vrot) & 7) * 64 + 16 * i) * 1024, 0, 0);
  };
  const u64 sptr = (u64)(ws + (size_t)g * 262144);
  const u32x4 srd = {(unsigned)sptr, (unsigned)(sptr >> 32) & 0xffffu, 262144u, 0x00020000u};
  const unsigned lds0 = (unsigned)(size_t)smem;
  auto sdst = [&](auto PP, auto I) AI { CI(P, PP); CI(i, I); return P == 2 ? HT_OFF + w3 * HREG + i * 1024 : sbuf(P, i >> 2) + (i & 3) * 4096 + w3 * 1024; };
  auto sload = [&](auto PP) AI {
    CI(P, PP);
    sfor<0, 8>([&](auto I) AI {
      CI(i, I);
      dma_sc1(lds0 + sdst(PP, I), lane * 16, srd, ((4 * P + 2 * rh + ((i & 3) >> 1)) * 16 + ((8 * (i >> 2) + 4 * (i & 1) + w3 + 2 * vrot) & 15)) * 1024);
    });
  };
  auto sempty = [&](auto PP) AI {
    unsigned m = 0;
    sfor<0, 8>([&](auto I) AI {
      const u32x4 v = *(LAS u32x4*)(smem + sdst(PP, I) + lane * 16);
      m = umax_(umax_(m, v[0]), v[1]); m = umax_(umax_(m, v[2]), v[3]);
    });
    return __any(m == EMPTY);
  };
  auto scheck = [&](auto PP) AI {
    unsigned spins = 0;
#pragma unroll 1
    while (sempty(PP) && ++spins < (1u << 16)) { sload(PP); waitvm<0>(); }
    asm volatile("" ::: "memory");
  };
  auto signal = [&](int w) {
    waitlgkm<0>();
    if (lane == 0) lds_inc(misc + w * 4);
  };
  auto await = [&](int w, unsigned target = 4) {
    volatile LAS unsigned* words = (volatile LAS unsigned*)(smem + MISC_OFF);
#pragma unroll 1
    for (unsigned spins = 0; spins < (1u << 24); spins++) {
      if ((unsigned)__builtin_amdgcn_readfirstlane(words[w]) == target) break;
      __builtin_amdgcn_s_sleep(1);
    }
    asm volatile("" ::: "memory");
  };

  if (wave < 4) {
    const int ng = wave;
    __builtin_amdgcn_s_setprio(2);
    const auto rout = __builtin_amdgcn_make_buffer_rsrc((void*)(out + (size_t)batch * (T_ * 1024)), 0, T_ * 1024, 0x00020000);
    waitvm<4>();
    {
      LAS char* tb = smem + TAB_OFF + ng * 16 * TS;
      unsigned tap[13];
#pragma unroll
      for (int m = 0; m < 13; m++) {
        const unsigned o = 12 * q - 9 + m;
        tap[m] = *(LAS unsigned short*)(tb + (o < 31u ? o : 31u) * 32 + l15 * 2);
      }
      waitlgkm<0>();
      LAS unsigned* tw = (LAS unsigned*)(tb + 4 + l15 * TS) + 6 * q;
#pragma unroll
      for (int m = 0; m < 12; m++) tw[(m & 1) ? 19 + (m >> 1) : (m >> 1) - 1] = tap[m] | (tap[m + 1] << 16);
      asm volatile("" ::: "memory");
      if (threadIdx.x < 16) *(LAS unsigned*)(smem + MISC_OFF + threadIdx.x * 4) = 0u;
    }
    __builtin_amdgcn_s_waitcnt(0x0F70);
    const u64 ep0 = __hip_atomic_load(lane == 0 ? epoch : (GAS u64*)(ws + WS_HEPOCH + (g * 8 + j + (lane == 2 ? 8 : 0)) * 128), RLX_AGENT);
    const Base3 abase(smem + (l15 >> 1) * 1024 + (l15 >> 2) * 64 + (l15 & 1) * 512 + ((q ^ (l15 & 3)) * 16));
    constexpr int RH = 8 * 1024 + 4 * 64;
    const int q4 = l15 >> 2, p4 = lane & 3;
    const Base2 wa(smem + (8 * q + q4) * 256 + ((ng ^ q4 ^ ((q & 1) << 2)) * 32) + p4 * 8);
    const Base2 wg(smem + (8 * q + q4) * 256 + (((4 + ng) ^ q4 ^ ((q & 1) << 2)) * 32) + p4 * 8);
    LAS char* hbase = opaque(smem + HT_OFF + (ng * 16 + l15) * HS + q * 16);
    bf16x8 Ba[16], Bg[16];
    bf16x8 A[PF][2];
    f32x4 ca[3][2], cgt[3][2];
    float e[8];
    u32x4 hp, hp0;

    auto loadW = [&](auto R) {
      CI(r, R);
      constexpr int ks = r >> 1, off = slot_of(wunit(ks >> 1)) + (ks & 1) * 8192;
      if constexpr (r & 1) Bg[ks] = cat8(ldtr(wg.at<off>()), ldtr(wg.at<off + 1024>()));
      else Ba[ks] = cat8(ldtr(wa.at<off>()), ldtr(wa.at<off + 1024>()));
    };
    auto glu = [&](auto U, auto PP, auto BP) {
      CI(u0, U); CI(pp, PP); CI(bp, BP);
      constexpr int u = u0 == 0 ? 0 : u0 == 1 ? -1 : u0 - 1;
      if constexpr (u >= 0 && u < 4) { e[2 * u] = vnl2e(cgt[pp][u >> 1][(2 * u) & 3]); e[2 * u + 1] = vnl2e(cgt[pp][u >> 1][(2 * u + 1) & 3]); }
      if constexpr (u >= 4 && u < 12) e[u - 4] = vexp2(e[u - 4]);
      if constexpr (u >= 12 && u < 16) { e[2 * u - 24] = vadd1(e[2 * u - 24]); e[2 * u - 23] = vadd1(e[2 * u - 23]); }
      if constexpr (u >= 16 && u < 24) e[u - 16] = vrcp(e[u - 16]);
      if constexpr (u >= 24 && u < 28) {
        constexpr int i = 2 * u - 48;
        e[i] = vmul(e[i], ca[pp][i >> 2][i & 3]); e[i + 1] = vmul(e[i + 1], ca[pp][i >> 2][(i & 3) + 1]);
      }
      if constexpr (u == 28 || u == 29) { constexpr int i = 2 * u - 56; hp[i] = vpk(e[2 * i], e[2 * i + 1]); hp[i + 1] = vpk(e[2 * i + 2], e[2 * i + 3]); }
      if constexpr (u == 30) { if constexpr (bp == 0) hp0 = hp; else *(LAS u32x4*)(hbase + (blk(bp) & 7) * 64) = hp; }
    };
    auto step = [&](auto I, auto PP, auto KS, auto FIRST, auto NOFF, auto&& f0, auto&& f1) {
      CI(i, I); CI(pp, PP); CI(ks, KS); CI(first, FIRST); CI(noff, NOFF);
      if constexpr (first) mf0(ca[pp][0], A[i][0], Ba[ks]); else mf(ca[pp][0], A[i][0], Ba[ks]);
      sfence(); f0(); sfence();
      if constexpr (first) mf0(cgt[pp][0], A[i][0], Bg[ks]); else mf(cgt[pp][0], A[i][0], Bg[ks]);
      sfence();
      if constexpr (noff >= 0) A[i][0] = ldsA(abase.at<noff>());
      sfence();
      if constexpr (first) mf0(cgt[pp][1], A[i][1], Bg[ks]); else mf(cgt[pp][1], A[i][1], Bg[ks]);
      sfence(); f1(); sfence();
      if constexpr (first) mf0(ca[pp][1], A[i][1], Ba[ks]); else mf(ca[pp][1], A[i][1], Ba[ks]);
      sfence();
      if constexpr (noff >= 0) A[i][1] = ldsA(abase.at<noff + RH>());
      sfence();
    };
    auto nop = [] {};
    auto primeA = [&](auto N) {
      CI(n, N);
      sfor<0, PF>([&](auto I) { CI(i, I); A[i][0] = ldsA(abase.at<slot_of(n) + i * 64>()); A[i][1] = ldsA(abase.at<slot_of(n) + i * 64 + RH>()); });
    };

    waitlgkm<0>();
    ST(4);
    sfor<0, 2>([&](auto HH) {
      CI(hf, HH);
      auto aoff = [](int s) { return s < 16 ? slot_of(4 + 6 * hf + ((s >> 1) & 1)) + (2 * (s >> 2) + (s & 1)) * 64 : hf ? slot_of(12) + (s - 16) * 64 : -1; };
      sfor<0, 16>([&](auto S) {
        CI(s, S);
        if constexpr (s == 0) {
          bar();
          ST(1 + 2 * hf);
          sfor<16 * hf, 16 * hf + 4>(loadW);
          sfor<0, PF>([&](auto I) { CI(i, I); A[i][0] = ldsA(abase.at<aoff(i)>()); A[i][1] = ldsA(abase.at<aoff(i) + RH>()); });
        }
        if constexpr ((s & 3) == 2 && s < 12) bar();
        if constexpr (hf == 1 && s == 16 - PF) bar();
        step(IC<s % PF>{}, IC<(s >> 1) & 1>{}, IC<8 * hf + 2 * (s >> 2) + (s & 1)>{}, IC<hf == 0 && (s == 0 || s == 2)>{}, IC<aoff(s + PF)>{},
             [&] { if constexpr ((s & 2) && s < 12) loadW(IC<16 * hf + (s & ~3) + 4 + 2 * (s & 1)>{}); },
             [&] { if constexpr ((s & 2) && s < 12) loadW(IC<16 * hf + (s & ~3) + 5 + 2 * (s & 1)>{}); });
      });
      if constexpr (hf == 0) ST(2);
    });
    ST(5); STR(0);
    const Base2 vb(smem + (8 * q + q4) * 256 + ((ng ^ q4) * 64) + p4 * 16);
    auto loadV = [&](auto R) {
      CI(r, R);
      constexpr int ks = r >> 1, off = slot_of(24 + (ks >> 1)) + (ks & 1) * 8192 + (r & 1) * 8;
      if constexpr (r & 1) Bg[ks] = cat8(ldtr(vb.at<off>()), ldtr(vb.at<off + 1024>()));
      else Ba[ks] = cat8(ldtr(vb.at<off>()), ldtr(vb.at<off + 1024>()));
    };
    sfor<2, 6>([&](auto BB) {
      CI(b, BB);
      sfor<0, 16>([&](auto S) {
        CI(s, S);
        constexpr int n = s + PF, gs = 16 * (b - 1) + s;
        if constexpr (s == 3) { waitlgkm<6>(); bar(); }
        CST
        auto fill = [&](auto G) {
          CI(gp, G);
          if constexpr (b == 2) {
            if constexpr (gp >= 1 && gp < 17) { glu(IC<2 * gp - 2>{}, IC<0>{}, IC<0>{}); glu(IC<2 * gp - 1>{}, IC<0>{}, IC<0>{}); }
            if constexpr (gp == 16) glu(IC<0>{}, IC<1>{}, IC<1>{});
            if constexpr (gp >= 17) { glu(IC<2 * gp - 33>{}, IC<1>{}, IC<1>{}); glu(IC<2 * gp - 32>{}, IC<1>{}, IC<1>{}); }
          } else {
            glu(IC<gp + 31>{}, IC<(b + 1) % 3>{}, IC<b - 2>{});
            glu(IC<gp - 1>{}, IC<(b + 2) % 3>{}, IC<b - 1>{});
          }
          if constexpr (b == 3 && gp == 2) *(LAS u32x4*)(hbase + (blk(0) & 7) * 64) = hp0;
          if constexpr (b == 2 && gp == 0) {
            const u64 tg = opaque64(ep0) + 0x9E3779B97F4A7C15ull;
            if (threadIdx.x < 3) *(LAS u64*)(smem + MISC_OFF + 64 + threadIdx.x * 8) = tg;
          }
        };
        step(IC<gs % PF>{}, IC<b % 3>{}, IC<s>{}, IC<s == 0>{}, IC<slot_of(8 + 2 * b + (n >> 3)) + (n & 7) * 64>{},
             [&] { fill(IC<2 * s>{}); }, [&] { fill(IC<2 * s + 1>{}); });
      });
    });
    sfor<0, 32>([&](auto S) {
      CI(s, S);
      constexpr int n = s + PF, gs = 80 + s, set = (s >> 3) & 1, ks = (s >> 4) * 8 + (s & 7);
      if constexpr (s == 3) waitlgkm<6>();
      if constexpr (s == 3 || s == 19) bar();
      auto fill = [&](auto G) {
        CI(gp, G);
        glu(IC<gp + 31>{}, IC<1>{}, IC<4>{});
        glu(IC<gp - 1>{}, IC<2>{}, IC<5>{});
        if constexpr (gp >= 38 && gp < 50) loadV(IC<gp - 38>{});
        if constexpr (gp >= 50) glu(IC<gp - 50>{}, IC<0>{}, IC<6>{});
      };
      step(IC<gs % PF>{}, IC<set>{}, IC<ks>{}, IC<ks == 0>{}, IC<(n < 32 ? slot_of(20 + (n >> 3)) + (n & 7) * 64 : -1)>{},
           [&] { fill(IC<2 * s>{}); }, [&] { fill(IC<2 * s + 1>{}); });
    });
    ST(6);
    sfor<14, 32>([&](auto U) { glu(U, IC<0>{}, IC<6>{}); });
    sfor<0, 32>([&](auto U) { glu(U, IC<1>{}, IC<7>{}); });
    waitlgkm<0>();
    ST(17);
    bar();
    ST(7); STR(1);
    const Base3 sbase(smem + lane * 16);
    const int ovo = (t0 - 15 + rh * 64 + l15) * 1024 + cg * 256 + ng * 64 + q * 16;
    unsigned op[4];
    auto ostep = [&](auto U, auto PP, auto PC) {
      CI(u, U); CI(pp, PP); CI(pc, PC);
      constexpr int hf = u / 3, s = u % 3;
      if constexpr (u < 6) {
        if constexpr (s == 0) { op[0] = vpk(ca[pp][hf][0], ca[pp][hf][1]); op[1] = vpk(ca[pp][hf][2], ca[pp][hf][3]); }
        if constexpr (s == 1) { op[2] = vpk(cgt[pp][hf][0], cgt[pp][hf][1]); op[3] = vpk(cgt[pp][hf][2], cgt[pp][hf][3]); }
        if constexpr (s == 2) __builtin_amdgcn_raw_buffer_store_b128((u32x4){op[0], op[1], op[2], op[3]}, rout, ovo + ((1 - (pc >> 1)) * 128 + hf * 32 + (pc & 1) * 16) * 1024, 0, OAUX);
      }
    };
    CSLP
    sload(IC<0>{});
    sfor<12, 20>(loadV);
    sfence();
    sfor<0, 4>([&](auto I) { CI(i, I); issueV(29, i); });
    __builtin_amdgcn_s_waitcnt(0x0070);
    waitvm<0>();
    ST(14);
    sload(IC<1>{});
    scheck(IC<0>{});
    ST(15);
    bar();
    ST(8);
    sfor<0, PF>([&](auto I) { CI(i, I); A[i][0] = ldsA(sbase.at<sfrag(0, 0, i)>()); A[i][1] = ldsA(sbase.at<sfrag(0, 1, i)>()); });
    sfor<0, 64>([&](auto S) {
      CI(gs, S);
      constexpr int P = gs >> 4, s = gs & 15, i = gs % PF, pp = P & 1, n = gs + PF;
      if constexpr (gs == B11S || n == 32 || n == 48) {
        if constexpr (gs == B11S) { waitvm<0>(); scheck(IC<1>{}); }
        bar();
        ST(9 + (gs >> 4));
      }
      if constexpr (s == 0) mf0(ca[pp][0], Ba[s], A[i][0]); else mf(ca[pp][0], Ba[s], A[i][0]);
      sfence();
      if constexpr (P > 0 && s >= 2) ostep(IC<s - 2>{}, IC<pp ^ 1>{}, IC<P - 1>{});
      if constexpr (gs < 2) loadV(IC<20 + 2 * gs>{});
      if constexpr (gs >= B11S && gs < B11S + 4) loadV(IC<24 + 2 * (gs - B11S)>{});
      sfence();
      if constexpr (s == 0) mf0(cgt[pp][0], Bg[s], A[i][0]); else mf(cgt[pp][0], Bg[s], A[i][0]);
      sfence();
      if constexpr (n < 64) A[i][0] = ldsA(sbase.at<sfrag((n & 63) >> 4, 0, n & 15)>());
      sfence();
      if constexpr (s == 0) mf0(cgt[pp][1], Bg[s], A[i][1]); else mf(cgt[pp][1], Bg[s], A[i][1]);
      sfence();
      if constexpr (gs < 2) loadV(IC<21 + 2 * gs>{});
      if constexpr (gs >= B11S && gs < B11S + 4) loadV(IC<25 + 2 * (gs - B11S)>{});
      sfence();
      if constexpr (s == 0) mf0(ca[pp][1], Ba[s], A[i][1]); else mf(ca[pp][1], Ba[s], A[i][1]);
      sfence();
      if constexpr (n < 64) A[i][1] = ldsA(sbase.at<sfrag((n & 63) >> 4, 1, n & 15)>());
      sfence();
    });
    ST(12);
    nops<7>();
    sfor<0, 6>([&](auto U) { ostep(U, IC<1>{}, IC<3>{}); nops<1>(); });
    ST(13);
    STDUMP();
  } else {
    const int hw = wave - 4;
    auto issue1 = [&](int n, int i) {
      if (n < 4 || (n >= 6 && n < 10)) {
        const int m = n < 4 ? n : n - 2;
        __builtin_amdgcn_raw_ptr_buffer_load_lds(rw1, smem + slot_of(n) + hw * 1024 + i * 4096, 16, vw, (((m & 4) + ((m + KROT) & 3)) * 64 + 16 * i) * 2048, 0, 0);
      } else if (n < 24) {
        const int b = n == 4 || n == 10 ? 0 : n == 5 || n == 11 ? 1 : n < 20 ? (n - 8) >> 1 : 6 + (n & 1);
        const int half = n < 6 ? 0 : n < 12 ? 1 : n < 20 ? (n & 1) : (n - 20) >> 1;
        __builtin_amdgcn_raw_ptr_buffer_load_lds(rx, smem + slot_of(n) + hw * 1024 + (hw >> 1) * 64 + i * (4096 + 128), 16, vx,
                                                 (blk(b) - 1) * 32768 + half * 512 + (16 * (i & 1) + 4 * (i >> 1)) * 1024, 0, 0);
      } else {
        issueV(n, i);
      }
    };
    auto issue = [&](int n) {
#pragma unroll
      for (int i = 0; i < 4; i++) issue1(n, i);
      asm volatile("" ::: "memory");
    };
    STA;
    bf16x8 T1[16], T2[8];
    auto loadT = [&](auto G) {
      LAS char* tb = smem + TAB_OFF + hw * 16 * TS;
      const int b7 = lane & 7, h1 = l15 >> 3;
      auto idx = [](int o) { return (o & 1) ? (o + 7) >> 1 : 23 + (o >> 1); };
      CI(gg, G);
      LAS unsigned* p = (LAS unsigned*)opaque(tb + 4 + (8 * h1 + 4 * (gg & 1)) * TS + (gg < 2 ? idx(8 * q - b7 - 2) : q == h1 ? idx(30 - b7) : 19) * 4);
      sfor<0, 4>([&](auto QD) {
        CI(qd, QD);
        const u32x4 v = {p[qd * 44], p[qd * 44 + 1], p[qd * 44 + 2], p[qd * 44 + 3]};
        sfence();
        if constexpr (gg < 2) T1[4 * gg + qd] = __builtin_bit_cast(bf16x8, v); else T2[4 * (gg - 2) + qd] = __builtin_bit_cast(bf16x8, v);
      });
    };
    auto splitT = [&](auto PR) {
      CI(pr, PR);
      const unsigned mh = 0u - (unsigned)(l15 >> 3);
      u32x4 v = __builtin_bit_cast(u32x4, T1[pr]);
      u32x4 a = v & ~mh, b = v & mh;
      asm volatile("" : "+v"(a), "+v"(b));
      T1[pr] = __builtin_bit_cast(bf16x8, a); T1[pr + 8] = __builtin_bit_cast(bf16x8, b);
    };
    issue(5); issue(1); waitvm<4>(); STA; bar();
    issue(2); issue(3); waitvm<8>(); bar();
    issue(6); waitvm<8>(); bar();
    issue(10); waitvm<8>(); STA; bar();
    sfor<0, 4>(loadT);
    STA;
    issue(11); issue(7); waitvm<4>(); STA; bar();
    issue(8); issue(9); waitvm<8>(); bar();
    issue(12); waitvm<8>(); bar();
    issue(13); waitvm<8>(); STA; bar();
    issue(14); waitvm<8>(); STA; bar();
    u32x4 empty4 = {EMPTY, EMPTY, EMPTY, EMPTY};
    asm volatile("" : "+v"(empty4));
    auto mark = [&](int i) { __builtin_amdgcn_raw_buffer_store_b128(empty4, rs, svo + (i & 1) * 128, (i >> 1) * 65536, 16); asm volatile("" ::: "memory"); };
    issue(15); issue(16);
    waitvm<8>();
    STA;
    bar();
    STA;
    issue(17); issue(18); mark(0); mark(1);
    sfor<0, 4>(splitT);
    waitvm<10>();
    STA;
    bar();
    STA;
    issue(19); issue(20); mark(2); mark(3);
    sfor<4, 8>(splitT);
    u64 tag = 0;
    auto lo = [] { sfence(); __builtin_amdgcn_s_setprio(0); sfence(); };
    auto hi = [] { sfence(); __builtin_amdgcn_s_setprio(3); sfence(); };
    f32x4 d[8];
    auto convmm = [&](auto HH) AI {
      CI(H, HH);
      constexpr int NW = 9;
      bf16x8 W[NW];
      LAS char* c1 = opaque(smem + HT_OFF + hw * HREG + ((16 * (1 - H) + l15 + q) & 31) * 16);
      LAS char* c2 = opaque(smem + HT_OFF + hw * HREG + (q & 1) * 8 * HS + ((16 * (1 - H) + l15 + 4) & 31) * 16);
      auto rd = [&](auto K) { CI(k, K); constexpr int pr = k / 3, e = k % 3; W[k % NW] = ldsA((e < 2 ? c1 + (pr + 8 * e) * HS : c2 + pr * HS)); sfence(); };
      sfor<0, NW>(rd);
      sfor<0, 24>([&](auto K) {
        CI(k, K);
        constexpr int pr = k / 3, e = k % 3;
        d[pr] = __builtin_amdgcn_mfma_f32_16x16x32_bf16(W[k % NW], e < 2 ? T1[(pr + 8 * e) & 15] : T2[pr], e == 0 ? (f32x4){0.f, 0.f, 0.f, 0.f} : d[pr], 0, 0, 0);
        sfence();
        if constexpr (k + NW < 24) rd(IC<k + NW>{});
      });
    };
    auto silu = [&](auto HH, auto R) AI {
      {
        CI(H, HH); CI(r, R);
        float t[8];
        sfor<0, 8>([&](auto I) { CI(i, I); t[i] = d[i][r] * -1.4426950408889634f; sfence(); });
        sfor<0, 8>([&](auto I) { CI(i, I); t[i] = vexp2(t[i]); });
        sfor<0, 8>([&](auto I) { CI(i, I); t[i] = vadd1(t[i]); });
        sfor<0, 8>([&](auto I) { CI(i, I); t[i] = vrcp(t[i]); });
        sfor<0, 8>([&](auto I) { CI(i, I); t[i] = vmul(t[i], d[i][r]); });
        u32x4 v = {pk2(t[0], t[1]), pk2(t[2], t[3]), pk2(t[4], t[5]), pk2(t[6], t[7])};
        v = (u32x4){umin_(v[0], EMPTY - 1), umin_(v[1], EMPTY - 1), umin_(v[2], EMPTY - 1), umin_(v[3], EMPTY - 1)};
        __builtin_amdgcn_raw_buffer_store_b128(v, rs, svo + (2 * H + (r >> 1)) * 65536 + (r & 1) * 128, 0, 16);
        asm volatile("" ::: "memory");
        sfence();
      }
    };
    const bool hin = (g & 15) != 0, hout = (g & 15) != 15;
    const auto rhalo = __builtin_amdgcn_make_buffer_rsrc((void*)(ws + WS_HALO + (size_t)(g * 8 + j) * 4096), 0, hout ? 9 * 4096 : 4096, 0x00020000);
    LAS char* hh = smem + HT_OFF + hw * HREG + (lane >> 2) * HS + (lane & 3) * 16;
    waitvm<12>();
    STA;
    bar();
    STA;
    __builtin_amdgcn_raw_buffer_store_b128(*(LAS u32x4*)hh, rhalo, 8 * 4096 + hw * 1024 + lane * 16, 0, 16);
    asm volatile("" ::: "memory");
    issue(21); issue(22); mark(4); mark(5);
    const int fl8 = lane < 8 || (lane == 8 && hin) || lane == 9 ? lane : 0;
    tag = *(LAS u64*)(smem + MISC_OFF + 64 + (fl8 < 8 ? 0 : fl8 - 7) * 8);
    GAS u64* fp = fl8 < 8 ? flags + fl8 * FST : (GAS u64*)(ws + WS_HFLAG + (g * 8 + j + (fl8 - 8) * 8) * 128);
    auto flagck = [&](GAS u64* p, u64 v, bool skip) AI {
      if (!__all(v == tag || skip)) {
        unsigned spins = 0;
#pragma unroll 1
        do { __builtin_amdgcn_s_sleep(PSL); v = __hip_atomic_load(p, RLX_AGENT); } while (!__all(v == tag || skip) && ++spins < (1u << 20));
      }
      asm volatile("" ::: "memory");
    };
    waitvm<10>();
    STA;
    bar();
    STA;
    if (hw == 0 && lane == 9 && hout) __hip_atomic_store(fp, tag, RLX_AGENT);
    mark(6); mark(7); issue(23); issue(24);
    waitvm<8>();
    STA;
    bar();
    STA;
    if (hw == 0 && lane == j) __hip_atomic_store(fp, tag, RLX_AGENT);
    issue(25); issue(26);
    GAS u64* fph = (GAS u64*)(ws + WS_HFLAG + (g * 8 + j) * 128);
    u64 fv = __hip_atomic_load(fph, RLX_AGENT);
    hi();
    convmm(IC<0>{});
    silu(IC<0>{}, IC<0>{});
    flagck(fph, fv, fl8 != 8);
    const u32x4 halo = __builtin_amdgcn_raw_buffer_load_b128(rhalo, hin ? hw * 1024 + lane * 16 : -1, 0, 16);
    asm volatile("" ::: "memory");
    sfor<1, 4>([&](auto R) { silu(IC<0>{}, R); });
    lo();
    waitvm<6>();
    STA;
    bar();
    STA;
    *(LAS u32x4*)hh = halo;
    issue(27); issue(28);
    flagck(fp, __hip_atomic_load(fp, RLX_AGENT), lane == 9);
    waitvm<0>();
    hi();
    STA;
    bar();
    STA;
    convmm(IC<1>{});
    sfor<0, 4>([&](auto R) { silu(IC<1>{}, R); });
    lo();
    STA;
    bar();
    STA;
    issue(30); issue(31);
    __builtin_amdgcn_s_sleep(SLPN);
    sload(IC<2>{});
    waitvm<8>();
    STA;
    bar();
    __builtin_amdgcn_s_sleep(SLP3);
    sload(IC<3>{});
    waitvm<8>();
    STA;
    scheck(IC<2>{});
    STA;
    bar();
    waitvm<0>();
    scheck(IC<3>{});
    STA;
    bar();
    if (hw == 0 && ((lane == 0 && j == 0) || (lane == 8 && hin)))
      __hip_atomic_store(lane == 0 ? epoch : (GAS u64*)(ws + WS_HEPOCH + (g * 8 + j) * 128), tag, RLX_AGENT);
    STDUMP();
  }
}

extern "C" void kernel_launch(void* const* d_in, const int* in_sizes, int n_in, void* d_out, int out_size, void* d_ws,
                              size_t ws_size, hipStream_t stream) {
  (void)hipFuncSetAttribute((const void*)conv_module_kernel, hipFuncAttributeMaxDynamicSharedMemorySize, LDS_BYTES);
  conv_module_kernel<<<256, 512, LDS_BYTES, stream>>>((const char*)d_in[0], (const char*)d_in[1], (const char*)d_in[2],
                                                      (const char*)d_in[3], (char*)d_out, (char*)d_ws);
}
```
